```python
import torch

def _softmax(x, axis=-1):
    return torch.softmax(x, dim=axis)
def _gelu(x):
    return torch.nn.functional.gelu(x, approximate="tanh")
def _silu(x):
    return torch.nn.functional.silu(x)
def _sigmoid(x):
    return torch.sigmoid(x)
def _layernorm(x, axis=-1, eps=1e-5):
    m = x.mean(dim=axis, keepdim=True)
    v = x.var(dim=axis, keepdim=True, unbiased=False)
    return (x - m) / torch.sqrt(v + eps)
def _scatter_add(tgt, idx, src, axis=0):
    return tgt.clone().index_add_(axis, idx, src)
def _segment_sum(src, seg, n):
    out = torch.zeros((n,) + src.shape[1:], dtype=src.dtype)
    return out.index_add_(0, seg, src)
def _pad(x, pw):
    flat = tuple(v for pair in reversed(pw) for v in pair)
    return torch.nn.functional.pad(x, flat)

kernel_name = 'sequential_preLN__ssm_conv_s4d__glu_gated_swiglu__prompt'

def setup_inputs():

    _w = (lambda a: (lambda t: t.to(torch.bfloat16) if t.is_floating_point() else t)(torch.as_tensor(a)))
    def _A_setup():
        Bsz, L, D, N = 1, 2048, 640, 128
        return {
            "u": _w((lambda *s: torch.randn(*s, dtype=torch.bfloat16) * 0.05)(Bsz, L, D)),
            "log_A_real": _w((lambda *s: torch.randn(*s, dtype=torch.bfloat16) * 6.4)(D, N) * 0.5),
            "A_imag": _w((lambda *s: torch.randn(*s, dtype=torch.bfloat16) * 0.05)(D, N) * 3.14),
            "C_real": _w((lambda *s: torch.randn(*s, dtype=torch.bfloat16) * 0.05)(D, N) / N ** 0.5),
            "C_imag": _w((lambda *s: torch.randn(*s, dtype=torch.bfloat16) * 0.05)(D, N) / N ** 0.5),
            "log_dt": _w((lambda *s: torch.randn(*s, dtype=torch.bfloat16) * 0.05)(D) * 0.5 - 4.0),
            "D_skip": _w((lambda *s: torch.randn(*s, dtype=torch.bfloat16) * 0.05)(D)),
        }
    _A_in = _A_setup()

    def _B_setup():
        B, S, D, F = 1, 2048, 640, 1728
        return {
            "x": _w((lambda *s: torch.randn(*s, dtype=torch.bfloat16) * 0.05)(B, S, D)),
            "w1": _w((lambda *s: torch.randn(*s, dtype=torch.bfloat16) * 0.4)(D, F) * (2.0 / D) ** 0.5),
            "w3": _w((lambda *s: torch.randn(*s, dtype=torch.bfloat16) * 0.4)(D, F) * (2.0 / D) ** 0.5),
            "w2": _w((lambda *s: torch.randn(*s, dtype=torch.bfloat16) * 0.4)(F, D) * (2.0 / F) ** 0.5),
        }
    _B_in = _B_setup()

    return {
        "A_u": _A_in["u"],
        "A_log_A_real": _A_in["log_A_real"],
        "A_A_imag": _A_in["A_imag"],
        "A_C_real": _A_in["C_real"],
        "A_C_imag": _A_in["C_imag"],
        "A_log_dt": _A_in["log_dt"],
        "A_D_skip": _A_in["D_skip"],
        "B_x": _B_in["x"],
        "B_w1": _B_in["w1"],
        "B_w3": _B_in["w3"],
        "B_w2": _B_in["w2"],
    }

def reference(A_u, A_log_A_real, A_A_imag, A_C_real, A_C_imag, A_log_dt, A_D_skip, B_x, B_w1, B_w3, B_w2):
    def _A_ref(u, log_A_real, A_imag, C_real, C_imag, log_dt, D_skip):
        L = u.shape[1]
        dt = torch.exp(log_dt)
        A = -torch.exp(log_A_real) + 1j * A_imag
        C = (C_real + 1j * C_imag) * dt[:, None]
        pos = (lambda x, dt: x.to(dt))(torch.arange(L), log_dt.dtype)
        P = torch.exp((A * dt[:, None])[:, :, None] * pos[None, None, :])
        K = 2.0 * torch.einsum("dn,dnl->dl", C, P).real
        L2 = 2 * L
        u_t = (lambda x, *d: x.permute(*d))(u, 0, 2, 1)
        Kf = (lambda x, *a, **k: torch.fft.rfft(x.float(), *a, **k))(K, L2)
        Uf = (lambda x, *a, **k: torch.fft.rfft(x.float(), *a, **k))(u_t, L2)
        y = (lambda x, *a, **k: torch.fft.irfft(x, *a, **k).to(torch.bfloat16))(Uf * Kf[None, :, :], L2)[:, :, :L]
        y = (lambda x, *d: x.permute(*d))(y, 0, 2, 1)
        return y + u * D_skip[None, None, :]

    def _B_ref(x, w1, w3, w2):
        gate = torch.einsum("bsd,df->bsf", x, w1)
        up = torch.einsum("bsd,df->bsf", x, w3)
        h = _silu(gate) * up
        out = torch.einsum("bsf,fd->bsd", h, w2)
        return out

    x = A_u
    h = x + _A_ref(_layernorm(x, axis=-1), A_log_A_real, A_A_imag, A_C_real, A_C_imag, A_log_dt, A_D_skip)
    h2 = h + _B_ref(_layernorm(h, axis=-1), B_w1, B_w3, B_w2)
    return h2
```

```cpp
#include <hip/hip_runtime.h>
#include <hip/hip_bf16.h>
#include <stdint.h>

#pragma clang diagnostic ignored "-Winline-asm"
#pragma clang diagnostic ignored "-Wunused-value"

namespace {

constexpr int SEQ = 2048, DM = 640, NMODE = 128, FF = 1728;

typedef _Float16 f16;
typedef f16 f16x2 __attribute__((ext_vector_type(2)));
typedef f16 f16x4 __attribute__((ext_vector_type(4)));
typedef f16 f16x8 __attribute__((ext_vector_type(8)));
typedef __bf16 b16;
typedef b16 b16x2 __attribute__((ext_vector_type(2)));
typedef b16 b16x4 __attribute__((ext_vector_type(4)));
typedef b16 b16x8 __attribute__((ext_vector_type(8)));
typedef short s16x4 __attribute__((ext_vector_type(4)));
typedef float f32x2 __attribute__((ext_vector_type(2)));
typedef float f32x4 __attribute__((ext_vector_type(4)));
typedef float f32x16 __attribute__((ext_vector_type(16)));
typedef unsigned u32;
typedef u32 u32x2 __attribute__((ext_vector_type(2)));
typedef u32 u32x4 __attribute__((ext_vector_type(4)));
#define LDS_AS __attribute__((address_space(3)))
typedef __attribute__((address_space(1))) unsigned long long gu64;
typedef __attribute__((address_space(1))) unsigned gu32;

constexpr size_t SZ_T    = (size_t)SEQ * DM * 2;
constexpr size_t OFF_UT  = 0;
constexpr size_t OFF_XT  = OFF_UT + SZ_T;
constexpr size_t OFF_KF  = OFF_XT + SZ_T;
constexpr size_t OFF_HT  = OFF_KF + SZ_T;
constexpr size_t OFF_W13 = OFF_HT + SZ_T;
constexpr size_t OFF_W2  = OFF_W13 + (size_t)108 * 40 * 1024;
constexpr size_t OFF_HM  = OFF_W2 + (size_t)8 * 54 * 5 * 1024;
constexpr size_t OFF_CS  = OFF_HM + (size_t)64 * 108 * 1024;
constexpr size_t OFF_KS  = OFF_CS + (size_t)108 * 32 * 4;
constexpr size_t OFF_SG  = OFF_KS + 640 * 4;
constexpr size_t OFF_FG  = OFF_SG + 16 * 256 * 8;
constexpr size_t OFF_FM  = OFF_FG + 16 * 1024;
constexpr size_t WS_END  = OFF_FM + 16 * 128;
constexpr int ZERO_GRANULES = (16 * 256 * 8 + 16 * 1024 + 16 * 128) / 16;

#define STAMP_DECL do {} while (0)
#define STAMP_ARG
#define STAMP_PASS
#define STAMP(i) do {} while (0)
#define STAMP_FLUSH(ws, k) do {} while (0)

#define DEADLINE(p) do {} while (0)
template <class T, class S> __device__ __forceinline__ T bc(S s) { return __builtin_bit_cast(T, s); }
__device__ __forceinline__ float lo16(u32 w) { return bc<float>(w << 16); }
__device__ __forceinline__ float hi16(u32 w) { return bc<float>(w & 0xffff0000u); }
__device__ __forceinline__ float bf2f(unsigned short u) { return bc<float>((u32)u << 16); }
__device__ __forceinline__ u32 pk_bf16(float a, float b) { f32x2 v = {a, b}; return bc<u32>(__builtin_convertvector(v, b16x2)); }
__device__ __forceinline__ u32 pk_f16(float a, float b) { f32x2 v = {a, b}; return bc<u32>(__builtin_convertvector(v, f16x2)); }
__device__ __forceinline__ float bf16r(float a) { return lo16(pk_bf16(a, a)); }
__device__ __forceinline__ int rfl(int v) { return __builtin_amdgcn_readfirstlane(v); }

template <int CTRL> __device__ __forceinline__ float dpp_add(float v) {
    return v + bc<float>(__builtin_amdgcn_update_dpp(0, bc<int>(v), CTRL, 0xf, 0xf, true));
}
__device__ __forceinline__ float row16_sum(float v) {
    v = dpp_add<0xB1>(v);
    v = dpp_add<0x4E>(v);
    v = dpp_add<0x141>(v);
    v = dpp_add<0x140>(v);
    return v;
}
__device__ __forceinline__ float wave_sum(float v) {
    v = row16_sum(v);
    v += __shfl_xor(v, 16);
    v += __shfl_xor(v, 32);
    return v;
}
__device__ __forceinline__ s16x4 tr_read(const char* p) {
    return __builtin_amdgcn_ds_read_tr16_b64_v4i16((LDS_AS s16x4*)p);
}
template <class T> __device__ __forceinline__ T cat8(s16x4 a, s16x4 b) {
    return bc<T>(__builtin_shufflevector(a, b, 0, 1, 2, 3, 4, 5, 6, 7));
}
__device__ __forceinline__ void dma16s_sc1(u32 slot, u32 off, u32 voff, __amdgpu_buffer_rsrc_t srd, u32 soff) {
    asm volatile("s_add_u32 m0, %0, %1\n\ts_nop 0\n\tbuffer_load_dwordx4 %2, %3, %4 offen sc1 lds"
                 :: "s"(slot), "s"(off), "v"(voff), "s"(srd), "s"(soff) : "m0", "scc", "memory");
}
__device__ __forceinline__ __amdgpu_buffer_rsrc_t make_srd(const void* p, u32 bytes) {
    return __builtin_amdgcn_make_buffer_rsrc((void*)p, (short)0, (int)bytes, 0x00020000);
}
#define ST_AUX 16
__device__ __forceinline__ void st16(__amdgpu_buffer_rsrc_t srd, u32 off, u32x4 v) { __builtin_amdgcn_raw_buffer_store_b128(v, srd, off, 0, ST_AUX); }
__device__ __forceinline__ void st8(__amdgpu_buffer_rsrc_t srd, u32 off, u32x2 v) { __builtin_amdgcn_raw_buffer_store_b64(v, srd, off, 0, ST_AUX); }
__device__ __forceinline__ void dma16(u32 lds, u32 voff, __amdgpu_buffer_rsrc_t srd, u32 soff) {
    asm volatile("s_mov_b32 m0, %0\n\ts_nop 0\n\tbuffer_load_dwordx4 %1, %2, %3 offen lds"
                 :: "s"(lds), "v"(voff), "s"(srd), "s"(soff) : "m0", "memory");
}
__device__ __forceinline__ void dma16s(u32 slot, u32 off, u32 voff, __amdgpu_buffer_rsrc_t srd, u32 soff) {
    asm volatile("s_add_u32 m0, %0, %1\n\ts_nop 0\n\tbuffer_load_dwordx4 %2, %3, %4 offen lds"
                 :: "s"(slot), "s"(off), "v"(voff), "s"(srd), "s"(soff) : "m0", "scc", "memory");
}
#define GAP() __builtin_amdgcn_sched_barrier(0)
#define WAIT_VM_LGKM_BAR(n) asm volatile("s_waitcnt vmcnt(" #n ") lgkmcnt(0)\n\ts_barrier" ::: "memory")

constexpr int KD_BYTES = 1536;
struct __attribute__((aligned(16))) IdxTab { unsigned short v[SEQ + 8]; };
constexpr int bf16pos(int l) {
    if (l < 256) return l;
    const int sh = (l < 512) ? 1 : (l < 1024) ? 2 : 3;
    int q = l >> sh; const int rem = l & ((1 << sh) - 1), half = 1 << (sh - 1);
    if (rem > half || (rem == half && (q & 1))) ++q;
    return q << sh;
}
constexpr IdxTab make_idx() {
    IdxTab t{};
    for (int l = -4; l < SEQ + 4; ++l) {
        if (l < 0 || l >= SEQ) { t.v[l + 4] = (unsigned short)KD_BYTES; continue; }
        const int p = bf16pos(l);
        int row = 0, b = 0;
        if (p < 256) { row = p & 7; b = p >> 3; }
        else if (p < 512) { const int o = p - 256; row = 8 + ((o & 7) >> 1); b = o >> 3; }
        else if (p < 1024) { const int o = p - 512, o2 = o & 255; row = 12 + 2 * (o >> 8) + ((o2 & 7) >> 2); b = o2 >> 3; }
        else if (p < 2048) { const int o = p - 1024; row = 16 + (o >> 8); b = (o & 255) >> 3; }
        else { row = 20; b = 0; }
        t.v[l + 4] = (unsigned short)((row * 32 + b) * 2);
    }
    return t;
}
__device__ const IdxTab IDX_TAB = make_idx();

constexpr int WARM_BLOCKS = 8;
constexpr int WARM_CODE   = 40960;
constexpr int K1_LN_PITCH = 1344;
constexpr int K1_KG_PITCH = 272;
constexpr int K1_KG_WAVE  = 53 * K1_KG_PITCH;
constexpr int K1_LDS      = 4 * K1_KG_WAVE;

__device__ __forceinline__ void ln1_block(int a, const u32x4* __restrict__ x, char* __restrict__ ws, char* smem) {
    STAMP_DECL; STAMP(0);
    const int tid = threadIdx.x, r = tid >> 4, c = tid & 15;
    const u32x4* row = x + (size_t)(16 * a + r) * 80;
    static_assert(ZERO_GRANULES == 128 * 25, "zeroing split");
    if (tid < 25) *(u32x4*)(ws + OFF_SG + (a * 25 + tid) * 16) = u32x4{0u, 0u, 0u, 0u};
    u32x4 v[5];
#pragma unroll
    for (int j = 0; j < 5; ++j) v[j] = row[j * 16 + c];
    float f[40];
    float s = 0.f, ss = 0.f;
#pragma unroll
    for (int j = 0; j < 5; ++j)
#pragma unroll
        for (int e = 0; e < 4; ++e) {
            const float lo = lo16(v[j][e]), hi = hi16(v[j][e]);
            f[j * 8 + 2 * e] = lo; f[j * 8 + 2 * e + 1] = hi;
            s += lo; ss = fmaf(lo, lo, ss); s += hi; ss = fmaf(hi, hi, ss);
        }
    const float mean = row16_sum(s) * (1.0f / DM);
    const float var = fmaxf(row16_sum(ss) * (1.0f / DM) - mean * mean, 0.f);
    STAMP(1);
    const float rinv = __builtin_amdgcn_rcpf(bf16r(__builtin_amdgcn_sqrtf(bf16r(bf16r(var) + 1e-5f))));
    const float mb = bf16r(mean);
    char* xs = smem; char* us = smem + 16 * K1_LN_PITCH;
#pragma unroll
    for (int j = 0; j < 5; ++j) {
        u32x4 uo;
#pragma unroll
        for (int e = 0; e < 4; ++e) {
            const u32 dd = pk_bf16(f[j * 8 + 2 * e] - mb, f[j * 8 + 2 * e + 1] - mb);
            const u32 qq = pk_bf16(lo16(dd) * rinv, hi16(dd) * rinv);
            uo[e] = pk_f16(lo16(qq), hi16(qq));
        }
        *(u32x4*)(xs + r * K1_LN_PITCH + (16 * j + c) * 16) = v[j];
        *(u32x4*)(us + r * K1_LN_PITCH + (16 * j + c) * 16) = uo;
    }
    __syncthreads();
    const __amdgpu_buffer_rsrc_t srd = make_srd(ws, (u32)WS_END);
    const int wave = tid >> 6, lane = tid & 63;
    const int half = lane >> 5, dblk = (lane >> 4) & 1, i = lane & 15, q = i >> 2, p = i & 3;
    const int lbase = (8 * half + q) * K1_LN_PITCH + (16 * dblk + 4 * p) * 2;
#pragma unroll
    for (int it = 0; it < 5; ++it) {
        const int d0 = 32 * (wave + 4 * it);
        const int d = d0 + 16 * dblk + i;
        const u32 go = (u32)((((d >> 2) * 128 + a) * 4 + (d & 3)) * 32 + 16 * half);
        const s16x4 x0 = tr_read(xs + lbase + d0 * 2), x1 = tr_read(xs + lbase + d0 * 2 + 4 * K1_LN_PITCH);
        const s16x4 u0 = tr_read(us + lbase + d0 * 2), u1 = tr_read(us + lbase + d0 * 2 + 4 * K1_LN_PITCH);
        st16(srd, (u32)OFF_XT + go, cat8<u32x4>(x0, x1));
        st16(srd, (u32)OFF_UT + go, cat8<u32x4>(u0, u1));
    }
    STAMP(2); asm volatile("s_waitcnt vmcnt(0)" ::: "memory"); STAMP(3);
    STAMP_FLUSH(ws, 0);
}

typedef f32x2 cplx;
struct cmulw { f32x2 rr, ii; };
__device__ __forceinline__ cmulw prepw(cplx w) { return {f32x2{w[0], w[0]}, f32x2{-w[1], w[1]}}; }
__device__ __forceinline__ cplx cmul(cplx a, cmulw w) { return __builtin_elementwise_fma(f32x2{a[1], a[0]}, w.ii, a * w.rr); }
__device__ __forceinline__ cplx csqr(cplx a) { return cmul(a, prepw(a)); }
__device__ __forceinline__ cplx cexp_p(float a2, float th, float tl, float p) {
    const float mag = __builtin_amdgcn_exp2f(a2 * p);
    const float rev = __builtin_amdgcn_fractf(th * p) + tl * p;
    return cplx{mag * __builtin_amdgcn_cosf(rev), mag * __builtin_amdgcn_sinf(rev)};
}

__device__ __forceinline__ void kgen_block(int blk, const unsigned short* __restrict__ logA, const unsigned short* __restrict__ Aim,
                                           const unsigned short* __restrict__ Cre, const unsigned short* __restrict__ Cim,
                                           const unsigned short* __restrict__ logdt, char* __restrict__ ws, char* smem) {
    STAMP_DECL; STAMP(0);
    const int tid = threadIdx.x, wave = tid >> 6, lane = tid & 63;
    const int ch = 2 * blk + (wave >> 1), n = (wave & 1) * 64 + lane;
    const float la = bf2f(logA[ch * NMODE + n]), ai = bf2f(Aim[ch * NMODE + n]);
    const float cr0 = bf2f(Cre[ch * NMODE + n]), ci0 = bf2f(Cim[ch * NMODE + n]);
    const float dt = bf16r(expf(bf2f(logdt[ch])));
    const float zr = -bf16r(expf(la)) * dt, zi = ai * dt;
    const float cr = cr0 * dt, ci = ci0 * dt;
    const float Bw = wave_sum(fabsf(cr) + fabsf(ci));
    STAMP(1);
    int ew = __builtin_amdgcn_frexp_expf(Bw); ew = min(max(ew, -100), 100);
    const float Sw = ldexpf(1.0f, 14 - ew), iSw = ldexpf(1.0f, ew - 14);
    const float a2 = zr * 1.44269504088896341f;
    const float INV2PI_HI = 0.15915493667125702f, INV2PI_LO = 6.4206382520e-09f;
    const float th = zi * INV2PI_HI;
    const float tl = fmaf(zi, INV2PI_HI, -th) + zi * INV2PI_LO;
    const cplx w1 = cexp_p(a2, th, tl, 1.f), w8 = cexp_p(a2, th, tl, 8.f), w256 = cexp_p(a2, th, tl, 256.f);
    const cplx w2 = csqr(w1), w4 = csqr(w2), w16 = csqr(w8);
    const cplx w512 = csqr(w256), w1024 = csqr(w512), w2048 = csqr(w1024);
    char* Hs = smem + wave * K1_KG_WAVE;
    char* Es = Hs + 21 * K1_KG_PITCH;
    auto putH = [&](int row, cplx v) { *(u32*)(Hs + row * K1_KG_PITCH + lane * 4) = pk_f16(v[0], -v[1]); };
    auto putE = [&](int row, cplx v) { *(u32*)(Es + row * K1_KG_PITCH + lane * 4) = pk_f16(v[0], v[1]); };
    const cplx c0 = {cr * Sw, ci * Sw};
    {
        const cmulw m1 = prepw(w1), m2 = prepw(w2), m4 = prepw(w4), m16 = prepw(w16), m256 = prepw(w256);
        cplx e0 = {1.f, 0.f}, e1 = w8;
#pragma unroll
        for (int b = 0; b < 32; b += 2) { putE(b, e0); putE(b + 1, e1); if (b < 30) { e0 = cmul(e0, m16); e1 = cmul(e1, m16); } }
        cplx h = c0;
#pragma unroll
        for (int k = 0; k < 8; ++k) { putH(k, h); if (k < 7) h = cmul(h, m1); }
        h = cmul(c0, m256);
#pragma unroll
        for (int k = 0; k < 4; ++k) { putH(8 + k, h); if (k < 3) h = cmul(h, m2); }
        const cplx c512 = cmul(c0, prepw(w512)), c768 = cmul(c512, m256);
        putH(12, c512); putH(13, cmul(c512, m4)); putH(14, c768); putH(15, cmul(c768, m4));
        h = cmul(c0, prepw(w1024));
#pragma unroll
        for (int k = 0; k < 4; ++k) { putH(16 + k, h); if (k < 3) h = cmul(h, m256); }
        putH(20, cmul(c0, prepw(w2048)));
    }
    STAMP(2);
    f32x16 acc = {};
    const int r = lane & 31, hh = lane >> 5;
    const char* ap = Hs + min(r, 20) * K1_KG_PITCH + 16 * hh;
    const char* bp = Es + r * K1_KG_PITCH + 16 * hh;
#pragma unroll
    for (int s = 0; s < 8; ++s)
        acc = __builtin_amdgcn_mfma_f32_32x32x16_f16(*(const f16x8*)(ap + 32 * s), *(const f16x8*)(bp + 32 * s), acc, 0, 0, 0);
    asm volatile("" :: "v"(acc[0])); STAMP(3);
    float* part = (float*)(smem + (wave | 1) * K1_KG_WAVE);
    if (wave & 1) {
#pragma unroll
        for (int i = 0; i < 12; ++i) part[i * 64 + lane] = acc[i] * iSw;
        if (lane == 0) part[12 * 64] = Bw;
    }
    __syncthreads();
    if (!(wave & 1)) {
        const float Bt = 2.0f * (Bw + part[12 * 64]);
        int eo = __builtin_amdgcn_frexp_expf(Bt); eo = min(max(eo, -100), 100);
        const float So = ldexpf(2.0f, 14 - eo);
        char* Kd = smem + wave * K1_KG_WAVE;
#pragma unroll
        for (int i = 0; i < 12; ++i) {
            const float kv = (acc[i] * iSw + part[i * 64 + lane]) * So;
            *(f16*)(Kd + (((i & 3) + 8 * (i >> 2) + 4 * hh) * 32 + r) * 2) = (f16)kv;
        }
        const __amdgpu_buffer_rsrc_t srd = make_srd(ws, (u32)WS_END);
        const u32 kd = (u32)OFF_KF + (u32)(ch * KD_BYTES + lane * 16);
        st16(srd, kd, *(const u32x4*)(Kd + lane * 16));
        if (lane < 32) st16(srd, kd + 1024, *(const u32x4*)(Kd + 1024 + lane * 16));
        if (lane == 0) ((float*)(ws + OFF_KS))[ch] = ldexpf(1.0f, eo - 14);
    }
    STAMP(4);
    STAMP(5);
    STAMP_FLUSH(ws, 0);
}

__global__ __launch_bounds__(256) void k1_ln_kgen(const u32x4* __restrict__ x, const unsigned short* __restrict__ logA,
                                                  const unsigned short* __restrict__ Aim, const unsigned short* __restrict__ Cre,
                                                  const unsigned short* __restrict__ Cim, const unsigned short* __restrict__ logdt,
                                                  char* __restrict__ ws
, const char* __restrict__ w1, const char* __restrict__ w3,
                                                  const char* __restrict__ w2, const char* __restrict__ outp) {
    extern __shared__ __attribute__((aligned(16))) char smem[];
    if (blockIdx.x >= 448) {
        u32 junk = 0;
        unsigned long long pc;
        asm volatile("s_getpc_b64 %0" : "=s"(pc));
        const char* c0 = (const char*)(pc & ~127ull);
        if (blockIdx.x < 448 + 8) {
            for (int o = threadIdx.x * 128; o < WARM_CODE; o += 256 * 128)
                asm volatile("global_load_dword %0, %1, off" : "+v"(junk) : "v"(c0 + o) : "memory");
            if (threadIdx.x < 34)
                asm volatile("global_load_dword %0, %1, off" : "+v"(junk) : "v"((const char*)IDX_TAB.v - 256 + threadIdx.x * 128) : "memory");
        }
        asm volatile("" :: "v"(junk));
        return;
    }
    if (blockIdx.x < 128) ln1_block(blockIdx.x, x, ws, smem);
    else kgen_block(blockIdx.x - 128, logA, Aim, Cre, Cim, logdt, ws, smem);
}

#define POLL_SLEEP 8
#define POLL_SLEEP2 4
#define PREP_DELAY 2
constexpr int K2_KCOPY = 4160;
constexpr int K2_KCH   = 4 * K2_KCOPY;
constexpr int K2_UCH   = 4640;
constexpr int K2_UOFF  = 4 * K2_KCH;
constexpr int K2_KD    = K2_UOFF + 4 * K2_UCH;
constexpr int K2_LDS   = K2_KD + 4 * 1552;

__device__ __forceinline__ void conv_block(int d4, const unsigned short* __restrict__ Dskip, char* __restrict__ ws, char* smem STAMP_ARG) {
    const int tid = threadIdx.x, wave = rfl(tid >> 6), lane = tid & 63;
    const int dch = wave & 3, hf = wave >> 2;
    const int d = 4 * d4 + dch;
    const int n = lane & 15, g = lane >> 4;
    const int tl = lane + 64 * hf;
    u32x4 kd = {0u, 0u, 0u, 0u};
    if (tl < 96) kd = *(const u32x4*)(ws + OFF_KF + (size_t)d * KD_BYTES + tl * 16);
    u32x4 uv[2];
#pragma unroll
    for (int j = 0; j < 2; ++j) uv[j] = *(const u32x4*)(ws + OFF_UT + (size_t)d4 * 16384 + (tid + 512 * j) * 16);
    u32x4 ixa[2]; u32x2 ixb[2];
#pragma unroll
    for (int j = 0; j < 2; ++j) {
        const int m = lane + 64 * (2 * hf + j);
        ixa[j] = *(const u32x4*)(IDX_TAB.v + 8 * m); ixb[j] = *(const u32x2*)(IDX_TAB.v + 8 * m + 8);
    }
    const float invS = ((const float*)(ws + OFF_KS))[d];
    const float dsk = bf2f(Dskip[d]);
    char* Kd = smem + K2_KD + dch * 1552;
    if (tl < 97) *(u32x4*)(Kd + tl * 16) = kd;
    char* Ub = smem + K2_UOFF;
    if (tid < 128) *(u32x4*)(Ub + (tid >> 5) * K2_UCH + (tid & 31) * 16) = u32x4{0u, 0u, 0u, 0u};
#pragma unroll
    for (int j = 0; j < 2; ++j) {
        const int G = tid + 512 * j, a = G >> 3, dc = (G >> 1) & 3, h2 = G & 1;
        *(u32x4*)(Ub + dc * K2_UCH + 512 + a * 32 + 16 * h2) = uv[j];
    }
    char* Kb = smem + dch * K2_KCH;
    if (hf == 0 && lane < 8) *(u32x4*)(Kb + (lane >> 1) * K2_KCOPY + (lane & 1) * 16) = u32x4{0u, 0u, 0u, 0u};
    __syncthreads();
    STAMP(10);
#pragma unroll
    for (int j = 0; j < 2; ++j) {
        const u32 ix[6] = {ixa[j][0], ixa[j][1], ixa[j][2], ixa[j][3], ixb[j][0], ixb[j][1]};
        u32 w[6];
#pragma unroll
        for (int e = 0; e < 6; ++e) {
            const u32 lo = *(const unsigned short*)(Kd + (ix[e] & 0xffffu)), hi = *(const unsigned short*)(Kd + (ix[e] >> 16));
            w[e] = lo | (hi << 16);
        }
        u32 a[5];
#pragma unroll
        for (int e = 0; e < 5; ++e) a[e] = __builtin_amdgcn_alignbit(w[e + 1], w[e], 16);
        char* o = Kb + 32 + (lane + 64 * (2 * hf + j)) * 16;
        *(u32x4*)(o)                = u32x4{w[2], w[3], w[4], w[5]};
        *(u32x4*)(o + K2_KCOPY)     = u32x4{a[1], a[2], a[3], a[4]};
        *(u32x4*)(o + 2 * K2_KCOPY) = u32x4{w[1], w[2], w[3], w[4]};
        *(u32x4*)(o + 3 * K2_KCOPY) = u32x4{a[0], a[1], a[2], a[3]};
    }
    u32x2 xv[4];
#pragma unroll
    for (int q = 0; q < 4; ++q) xv[q] = *(const u32x2*)(ws + OFF_XT + (size_t)d4 * 16384 + ((16 * (4 * hf + q) + n) * 4 + dch) * 32 + 8 * g);
    __syncthreads();
    STAMP(11);
    const int dd = g >> 1, h1 = g & 1, q4 = n >> 2, p = n & 3;
    const char* kb = Kb + q4 * K2_KCOPY + 2 * (16 + 16 * dd - 8 * h1 + 4 * p) - 8 + 256 * hf;
    const char* ub = Ub + dch * K2_UCH + 512 + 32 * (n - dd) + 16 * h1 - 64 * 3 - 256 * hf;
    f32x4 acc[8];
#pragma unroll
    for (int q = 0; q < 8; ++q) acc[q] = f32x4{0.f, 0.f, 0.f, 0.f};
    struct Fr { f16x8 a[8], b[8]; };
    auto load = [&](Fr& f, int rho) {
#pragma unroll
        for (int i = 0; i < 8; ++i) {
            f.a[i] = cat8<f16x8>(tr_read(kb + 64 * rho + 512 * i + 8), tr_read(kb + 64 * rho + 512 * i));
            f.b[i] = *(const f16x8*)(ub + 512 * i + 64 * (3 - rho));
        }
    };
    auto step = [&](const Fr& c, Fr& nx, int rho, bool ld) {
        s16x4 lo[8], hi[8];
        int m = 0;
#pragma unroll
        for (int s = 0; s < 8; ++s)
#pragma unroll
            for (int i = 0; i <= s; ++i) {
                acc[s] = __builtin_amdgcn_mfma_f32_16x16x32_f16(c.a[i], c.b[s - i], acc[s], 0, 0, 0);
                if (ld && m < 24) {
                    const int k = m / 3, w = m % 3;
                    if (w == 0) nx.b[k] = *(const f16x8*)(ub + 512 * k + 64 * (3 - rho));
                    else if (w == 1) lo[k] = tr_read(kb + 64 * rho + 512 * k + 8);
                    else hi[k] = tr_read(kb + 64 * rho + 512 * k);
                }
                ++m;
                GAP();
            }
        if (ld) {
#pragma unroll
            for (int k = 0; k < 8; ++k) nx.a[k] = cat8<f16x8>(lo[k], hi[k]);
        }
    };
    Fr f0, f1;
    load(f0, 0);
    step(f0, f1, 1, true);
    step(f1, f0, 2, true);
    step(f0, f1, 3, true);
    step(f1, f0, 0, false);
    __syncthreads();
    STAMP(12);
    f32x4* red = (f32x4*)smem;
#pragma unroll
    for (int q = 0; q < 4; ++q) red[(wave * 4 + q) * 64 + lane] = hf ? acc[q] : acc[4 + q];
    __syncthreads();
    const char* ue = Ub + dch * K2_UCH + 512 + 32 * n + 8 * g + 2048 * hf;
    char* Hs = smem + 32768;
#pragma unroll
    for (int q = 0; q < 4; ++q) {
        const f32x4 o = red[((wave ^ 4) * 4 + q) * 64 + lane];
        const f32x4 mine = hf ? acc[4 + q] : acc[q];
        const f16x4 uq = *(const f16x4*)(ue + 512 * q);
        float hv[4];
#pragma unroll
        for (int e = 0; e < 4; ++e) {
            const float yb = bf16r((mine[e] + o[e]) * invS);
            const float ud = bf16r((float)uq[e] * dsk);
            const float z = bf16r(yb + ud);
            const float xe = (e & 1) ? hi16(xv[q][e >> 1]) : lo16(xv[q][e >> 1]);
            hv[e] = xe + z;
        }
        *(u32x2*)(Hs + (16 * (4 * hf + q) + n) * 128 + dch * 32 + 8 * g) = u32x2{pk_bf16(hv[0], hv[1]), pk_bf16(hv[2], hv[3])};
    }
    __syncthreads();
    const __amdgpu_buffer_rsrc_t srd = make_srd(ws, (u32)WS_END);
#pragma unroll
    for (int j = 0; j < 2; ++j) {
        const int pc = wave + 8 * j;
        st16(srd, (u32)OFF_HT + (u32)((pc * 160 + d4) * 1024 + lane * 16), *(const u32x4*)(Hs + pc * 1024 + lane * 16));
    }
}

__device__ __forceinline__ void prep13_block(int j, const char* __restrict__ w1, const char* __restrict__ w3, char* __restrict__ ws, char* smem) {
    const int tid = threadIdx.x, wave = rfl(tid >> 6), lane = tid & 63;
    for (int i = 0; i < PREP_DELAY; ++i) __builtin_amdgcn_s_sleep(16);
    {
        u32x4 a[5], b[5];
#pragma unroll
        for (int it = 0; it < 5; ++it) {
            const int G = tid + 512 * it, k = G >> 2, c = G & 3;
            a[it] = *(const u32x4*)(w1 + (size_t)k * (FF * 2) + j * 64 + c * 16);
            b[it] = *(const u32x4*)(w3 + (size_t)k * (FF * 2) + j * 64 + c * 16);
        }
#pragma unroll
        for (int it = 0; it < 5; ++it) {
            const int G = tid + 512 * it;
            *(u32x4*)(smem + G * 32)      = u32x4{a[it][0], b[it][0], a[it][1], b[it][1]};
            *(u32x4*)(smem + G * 32 + 16) = u32x4{a[it][2], b[it][2], a[it][3], b[it][3]};
        }
    }
    __syncthreads();
    const int T = wave & 3, kh = wave >> 2;
    const int g = lane >> 4, q = (lane & 15) >> 2, p = lane & 3;
    const char* base = smem + (8 * g + q) * 128 + T * 32 + 8 * p + kh * 10 * 4096;
    float cs = 0.f;
    const b16x2 ones = {(b16)1.0f, (b16)1.0f};
    const __amdgpu_buffer_rsrc_t srd = make_srd(ws, (u32)WS_END);
#pragma unroll
    for (int ks = 0; ks < 10; ++ks) {
        const u32x4 v = cat8<u32x4>(tr_read(base + ks * 4096), tr_read(base + ks * 4096 + 512));
        st16(srd, (u32)OFF_W13 + (u32)(((4 * j + T) * 20 + 10 * kh + ks) * 1024 + lane * 16), v);
#pragma unroll
        for (int e = 0; e < 4; ++e) cs = __builtin_amdgcn_fdot2_f32_bf16(bc<b16x2>(v[e]), ones, cs, false);
    }
    cs += __shfl_xor(cs, 16); cs += __shfl_xor(cs, 32);
    float* red = (float*)(smem + 81920);
    if (lane < 16) red[wave * 16 + lane] = cs;
    __syncthreads();
    if (tid < 64) __hip_atomic_store((gu32*)(ws + OFF_CS) + 4 * j * 16 + tid, bc<u32>(red[tid] + red[64 + tid]), __ATOMIC_RELAXED, __HIP_MEMORY_SCOPE_AGENT);
}

__device__ __forceinline__ void prep2_block(int j, const char* __restrict__ w2, char* __restrict__ ws, char* smem) {
    const int tid = threadIdx.x, wave = rfl(tid >> 6), lane = tid & 63;
    for (int i = 0; i < PREP_DELAY; ++i) __builtin_amdgcn_s_sleep(16);
    const u32 lds0 = (u32)(size_t)smem;
    {
        const __amdgpu_buffer_rsrc_t s2 = make_srd(w2, DM * FF * 2);
#pragma unroll
        for (int it = 0; it < 10; ++it) {
            const int pc = wave + 8 * it, P = 64 * pc + lane, f = P / 80, c = (P % 80) ^ ((f & 7) << 1);
            dma16(lds0 + pc * 1024, f * 1280 + c * 16, s2, j * 64 * 1280);
        }
    }
    WAIT_VM_LGKM_BAR(0);
    const int g = lane >> 4, q = (lane & 15) >> 2, p = lane & 3;
    const __amdgpu_buffer_rsrc_t srd = make_srd(ws, (u32)WS_END);
#pragma unroll
    for (int sl = 0; sl < 2; ++sl)
#pragma unroll
        for (int it = 0; it < 5; ++it) {
            const int DT = wave * 5 + it;
            s16x4 r[2];
#pragma unroll
            for (int t = 0; t < 2; ++t) {
                const int f = 32 * sl + 8 * g + 4 * t + q, c = 2 * DT + (p >> 1);
                r[t] = tr_read(smem + (f * 80 + (c ^ ((f & 7) << 1))) * 16 + 8 * (p & 1));
            }
            st16(srd, (u32)OFF_W2 + (u32)((((DT / 5) * 54 + 2 * j + sl) * 5 + DT % 5) * 1024 + lane * 16), cat8<u32x4>(r[0], r[1]));
        }
}

constexpr int K3_STAGE = 16384 + 28 * 1024;
constexpr int K3_CS    = 3 * K3_STAGE;
constexpr int K3_STAT  = K3_CS + 1024;
constexpr int K3_SH    = K3_STAT + 2048;
constexpr int K3_SP    = K3_SH + 10240;
constexpr int K3_LDS   = K3_SP + 512;

struct G1Frag { b16x8 a[7]; b16x8 b[4]; };

template <int NT>
__device__ __forceinline__ void k3_consumer(char* __restrict__ ws, char* smem, int mt, int T0, int wave, int lane STAMP_ARG) {
    const int th = wave & 1, fh = wave >> 1;
    const int g = lane >> 4, q = (lane & 15) >> 2, p = lane & 3, tok = lane & 15;
    const int bo0 = 2 * g * 1024 + (4 * th + (g & 1)) * 128 + q * 32 + 8 * p;
    const int bo1 = 2 * g * 1024 + (4 * th - (g & 1)) * 128 + q * 32 + 8 * p;
#define BOFF(tt) ((((tt) & 1) ? bo1 : bo0) + 128 * (tt))
    const int aoff = 16384 + fh * NT * 2048 + lane * 16;
    f32x4 acc[4][NT];
#pragma unroll
    for (int tt = 0; tt < 4; ++tt)
#pragma unroll
        for (int i = 0; i < NT; ++i) acc[tt][i] = f32x4{0.f, 0.f, 0.f, 0.f};
    auto step = [&](const G1Frag& c, G1Frag& n, const char* sl, int ks) {
        s16x4 lo[4], hi[4];
#pragma unroll
        for (int i = 0; i < NT; ++i)
#pragma unroll
            for (int tt = 0; tt < 4; ++tt) {
                acc[tt][i] = __builtin_amdgcn_mfma_f32_16x16x32_bf16(c.a[i], c.b[tt], acc[tt][i], 0, 0, 0);
                const int m = 4 * i + tt;
                if (m >= 1 && m <= 8) {
                    const int r = m - 1;
                    if (r & 1) hi[r >> 1] = tr_read(sl + BOFF(r >> 1) + ks * 8192 + 1024);
                    else lo[r >> 1] = tr_read(sl + BOFF(r >> 1) + ks * 8192);
                } else if (m >= 9 && m < 9 + 2 * NT && ((m - 9) & 1) == 0) {
                    const int r = (m - 9) >> 1;
                    n.a[r] = *(const b16x8*)(sl + aoff + r * 2048 + ks * 1024);
                }
                GAP();
            }
#pragma unroll
        for (int tt = 0; tt < 4; ++tt) n.b[tt] = cat8<b16x8>(lo[tt], hi[tt]);
    };
    asm volatile("s_barrier" ::: "memory");
    STAMP(2);
    G1Frag f0, f1;
#pragma unroll
    for (int tt = 0; tt < 4; ++tt) f0.b[tt] = cat8<b16x8>(tr_read(smem + BOFF(tt)), tr_read(smem + BOFF(tt) + 1024));
#pragma unroll
    for (int i = 0; i < NT; ++i) f0.a[i] = *(const b16x8*)(smem + aoff + i * 2048);
    int slot = 0;
#pragma unroll 1
    for (int st = 0; st < 10; ++st) {
        step(f0, f1, smem + slot, 1);
        slot = (slot == 2 * K3_STAGE) ? 0 : slot + K3_STAGE;
        asm volatile("s_waitcnt lgkmcnt(0)\n\ts_barrier" ::: "memory");
        GAP();
        step(f1, f0, smem + slot, 0);
    }
    asm volatile("" :: "v"(acc[3][NT - 1][0])); STAMP(3);
    const __amdgpu_buffer_rsrc_t srd = make_srd(ws, (u32)WS_END);
    f32x4 cs[NT];
#pragma unroll
    for (int i = 0; i < NT; ++i) cs[i] = *(const f32x4*)(smem + K3_CS + (fh * NT + i) * 64 + g * 16);
#pragma unroll
    for (int tt = 0; tt < 4; ++tt) {
        const f32x4 st = *(const f32x4*)(smem + K3_STAT + (64 * th + 16 * tt + tok) * 16);
        const f32x2 nm2 = {st[0], st[0]}, ns2 = {st[1], st[1]}, sq2 = {st[2], st[2]};
        u32 v[8];
#pragma unroll
        for (int i = 0; i < NT; ++i) {
            const f32x2 a = __builtin_elementwise_fma(nm2, f32x2{cs[i][0], cs[i][1]}, f32x2{acc[tt][i][0], acc[tt][i][1]});
            const f32x2 b = __builtin_elementwise_fma(nm2, f32x2{cs[i][2], cs[i][3]}, f32x2{acc[tt][i][2], acc[tt][i][3]});
            const f32x2 t = a * ns2;
            const f32x2 ex = {__builtin_amdgcn_exp2f(t[0]), __builtin_amdgcn_exp2f(t[1])};
            const f32x2 dn = __builtin_elementwise_fma(ex, sq2, sq2);
            const f32x2 hm = (a * b) * f32x2{__builtin_amdgcn_rcpf(dn[0]), __builtin_amdgcn_rcpf(dn[1])};
            v[i] = bc<u32>(__builtin_convertvector(hm, b16x2));
        }
#pragma unroll
        for (int i = NT; i < 8; ++i) v[i] = 0u;
        const u32 dst = (u32)OFF_HM + (u32)(((mt * 8 + 4 * th + tt) * 216 + T0 + fh * NT + g) * 256 + tok * 16);
#pragma unroll
        for (int gr = 0; gr < 2; ++gr) {
            auto s02 = __builtin_amdgcn_permlane32_swap(v[4 * gr + 0], v[4 * gr + 2], false, false);
            auto s13 = __builtin_amdgcn_permlane32_swap(v[4 * gr + 1], v[4 * gr + 3], false, false);
            auto s01 = __builtin_amdgcn_permlane16_swap(s02[0], s13[0], false, false);
            auto s23 = __builtin_amdgcn_permlane16_swap(s02[1], s13[1], false, false);
            if (4 * gr + g < NT) st16(srd, dst + gr * 1024, u32x4{s01[0], s01[1], s23[0], s23[1]});
        }
    }
    STAMP(4);
    asm volatile("s_waitcnt vmcnt(0)\n\ts_barrier" ::: "memory");
    STAMP(5);
}

template <int NT>
__device__ __forceinline__ void k3_loader(char* __restrict__ ws, char* smem, int mt, int sl, int T0, int lw, int lane) {
    const u32 lds0 = (u32)(size_t)smem;
    const __amdgpu_buffer_rsrc_t srd = make_srd(ws, (u32)WS_END);
    const u32 voff = lane * 16;
    const u32 voffh0 = (lane >> 3) * 128 + (lane & 7) * 16, voffh1 = ((lane >> 3) ^ 1) * 128 + (lane & 7) * 16;
    constexpr int NW = NT;
    constexpr int NL = 4 + NW;
    u32 gh = (u32)OFF_HT + (u32)((mt * 160 + lw * 4) * 1024);
    u32 gw[NW];
#pragma unroll
    for (int i = 0; i < NW; ++i) { const int pc = lw * NW + i; gw[i] = (u32)OFF_W13 + (u32)(((T0 + (pc >> 1)) * 20 + (pc & 1)) * 1024); }
    const u32 lh = lw * 4096, lwo = 16384 + lw * NW * 1024;
    u32 islot = lds0;
    auto issue = [&]() {
#pragma unroll
        for (int i = 0; i < 4; ++i)
            dma16s_sc1(islot, lh + i * 1024, (i & 2) ? voffh1 : voffh0, srd, gh + i * 1024);
#pragma unroll
        for (int i = 0; i < NW; ++i) { dma16s_sc1(islot, lwo + i * 1024, voff, srd, gw[i]); gw[i] += 2048; }
        gh += 16384;
        islot = (islot == lds0 + 2 * K3_STAGE) ? lds0 : islot + K3_STAGE;
    };
    if (lw == 0) dma16s_sc1(lds0, K3_CS, voff, srd, (u32)OFF_CS + (u32)(T0 * 64));
    issue(); issue();
    {
        const u32 vo = (lane >> 3) * 1024 + (lane & 7) * 16;
#pragma unroll
        for (int i = 0; i < 3; ++i) {
            const int pc = min(lw + 4 * i, 9);
            dma16s_sc1(lds0, K3_SH + pc * 1024, vo, srd, (u32)OFF_HT + (u32)((mt * 160 + 80 * (sl & 1) + 8 * pc) * 1024 + (sl >> 1) * 128));
        }
    }
    asm volatile("s_waitcnt vmcnt(%0)\n\ts_barrier" :: "n"(NL + 3) : "memory");
    issue();
    asm volatile("s_waitcnt vmcnt(%0) lgkmcnt(0)\n\ts_barrier" :: "n"(NL) : "memory");
    issue();
    {
        const int g = lane >> 4, q = (lane & 15) >> 2, p = lane & 3;
        float s1 = 0.f, s2 = 0.f;
        const b16x2 ones = {(b16)1.0f, (b16)1.0f};
#pragma unroll
        for (int i = 0; i < 5; ++i) {
            const u32x2 w = bc<u32x2>(tr_read(smem + K3_SH + (20 * lw + 4 * i + g) * 128 + q * 32 + 8 * p));
#pragma unroll
            for (int e = 0; e < 2; ++e) {
                s1 = __builtin_amdgcn_fdot2_f32_bf16(bc<b16x2>(w[e]), ones, s1, false);
                s2 = __builtin_amdgcn_fdot2_f32_bf16(bc<b16x2>(w[e]), bc<b16x2>(w[e]), s2, false);
            }
        }
        s1 += __shfl_xor(s1, 16); s2 += __shfl_xor(s2, 16);
        s1 += __shfl_xor(s1, 32); s2 += __shfl_xor(s2, 32);
        if (lane < 16) *(f32x2*)(smem + K3_SP + (lw * 16 + lane) * 8) = f32x2{s1, s2};
    }
    gu64* grp = (gu64*)(ws + OFF_SG) + mt * 256;
    asm volatile("s_waitcnt vmcnt(%0) lgkmcnt(0)\n\ts_barrier" :: "n"(NL) : "memory");
    if (lw == 0 && lane < 16) {
        f32x2 t = *(const f32x2*)(smem + K3_SP + lane * 8);
#pragma unroll
        for (int w = 1; w < 4; ++w) t += *(const f32x2*)(smem + K3_SP + (w * 16 + lane) * 8);
        __hip_atomic_store(grp + sl * 16 + lane, ((unsigned long long)(bc<u32>(t[1]) | 0x80000000u) << 32) | bc<u32>(t[0]),
                           __ATOMIC_RELAXED, __HIP_MEMORY_SCOPE_AGENT);
    }
    issue();
#pragma unroll 1
    for (int st = 2; st < 7; ++st) {
        asm volatile("s_waitcnt vmcnt(%0) lgkmcnt(0)\n\ts_barrier" :: "n"(NL) : "memory");
        issue();
    }
    if (lw < 2) {
        const int t = 64 * lw + lane;
        const gu64* gp = grp + (t >> 4) * 32 + (t & 15);
        unsigned long long g0, g1;
        asm volatile("global_load_dwordx2 %0, %2, off sc1\n\tglobal_load_dwordx2 %1, %2, off offset:128 sc1" : "=&v"(g0), "=&v"(g1) : "v"(gp) : "memory");
        asm volatile("s_waitcnt vmcnt(%0) lgkmcnt(0)\n\ts_barrier" :: "n"(NL + 2) : "memory");
        asm volatile("s_waitcnt vmcnt(0) lgkmcnt(0)\n\ts_barrier" : "+v"(g0), "+v"(g1) :: "memory");
        bool ok = __all((g0 >> 63) && (g1 >> 63));
        for (int spin = 0; !ok && spin < 20000; ++spin) {
            __builtin_amdgcn_s_sleep(8);
            g0 = __hip_atomic_load(gp, __ATOMIC_RELAXED, __HIP_MEMORY_SCOPE_AGENT);
            g1 = __hip_atomic_load(gp + 16, __ATOMIC_RELAXED, __HIP_MEMORY_SCOPE_AGENT);
            ok = __all((g0 >> 63) && (g1 >> 63));
        }
        float s1 = bc<float>((u32)g0) + bc<float>((u32)g1);
        float s2 = bc<float>((u32)(g0 >> 32) & 0x7fffffffu) + bc<float>((u32)(g1 >> 32) & 0x7fffffffu);
        if (!ok) {
            s1 = 0.f; s2 = 0.f;
            const gu32* hp = (const gu32*)(ws + OFF_HT) + ((size_t)mt * 160 * 8 + (t >> 4)) * 32 + ((t & 15) >> 1);
            for (int d = 0; d < DM; ++d) {
                const u32 w = __hip_atomic_load(hp + (size_t)(d >> 2) * 256 + (d & 3) * 8, __ATOMIC_RELAXED, __HIP_MEMORY_SCOPE_AGENT);
                const float v = (t & 1) ? hi16(w) : lo16(w); s1 += v; s2 = fmaf(v, v, s2);
            }
        }
        const float mean = s1 * (1.0f / DM);
        const float var = fmaxf(s2 * (1.0f / DM) - mean * mean, 0.f);
        const float sg = bf16r(__builtin_amdgcn_sqrtf(bf16r(bf16r(var) + 1e-5f)));
        *(f32x4*)(smem + K3_STAT + t * 16) = f32x4{-bf16r(mean), -1.44269504088896341f * __builtin_amdgcn_rcpf(sg), sg * sg, 0.f};
    } else {
        asm volatile("s_waitcnt vmcnt(%0) lgkmcnt(0)\n\ts_barrier" :: "n"(NL) : "memory");
        asm volatile("s_waitcnt vmcnt(0) lgkmcnt(0)\n\ts_barrier" ::: "memory");
    }
    asm volatile("s_waitcnt lgkmcnt(0)\n\ts_barrier" ::: "memory");
}

constexpr int K4_STAGE = 18432;
constexpr int K4_NS    = 8;
constexpr int K4_HOFF  = K4_NS * K4_STAGE;
constexpr int K4_LDS   = K4_HOFF + 10240;

template <int NW>
__device__ __forceinline__ void k4_loader(char* __restrict__ ws, char* smem, int mt1, int sl1, int mt, int nt, int lw, int lane) {
    const u32 lds0 = (u32)(size_t)smem;
    const __amdgpu_buffer_rsrc_t srd = make_srd(ws, (u32)WS_END);
    const u32 voff = lane * 16;
    constexpr int F = NW + 2;
    const int pw0 = (lw < 2) ? lw * 3 : 6 + (lw - 2) * 2;
    u32 gh = (u32)OFF_HM + (u32)((mt * 4 + lw) * 216 * 256);
    u32 gw = (u32)OFF_W2 + (u32)((nt * 54 * 5 + pw0) * 1024);
    const u32 lh = lw * 2048, lwo = 8192 + pw0 * 1024;
    u32 hslot = lds0, wslot = lds0;
    auto next = [&](u32 sl) { return (sl == lds0 + (K4_NS - 1) * K4_STAGE) ? lds0 : sl + K4_STAGE; };
    auto issue_w = [&]() {
#pragma unroll
        for (int i = 0; i < NW; ++i) dma16s_sc1(wslot, lwo + i * 1024, voff, srd, gw + i * 1024);
        gw += 10240; wslot = next(wslot);
    };
    auto issue_h = [&]() {
#pragma unroll
        for (int i = 0; i < 2; ++i) dma16s_sc1(hslot, lh + i * 1024, voff, srd, gh + i * 1024);
        gh += 2048; hslot = next(hslot);
    };
#pragma unroll
    for (int s = 0; s < 7; ++s) issue_w();
    {
        const int d4l = lane >> 5, al = (lane >> 3) & 3, c = lane & 7;
        const u32 vo = (u32)((d4l * 8 + 4 * (mt & 1) + al) * 128 + c * 16);
#pragma unroll
        for (int i = 0; i < 3; ++i) {
            const int pc = min(lw + 4 * i, 9);
            dma16s_sc1(lds0, K4_HOFF + pc * 1024, vo, srd, (u32)OFF_HT + (u32)((((mt >> 1) * 160) + 20 * nt + 2 * pc) * 1024));
        }
    }
    asm volatile("s_barrier" ::: "memory");
    if (lw == 0) {
        gu32* fm = (gu32*)(ws + OFF_FM);
        if (lane == 0) __hip_atomic_store(fm + mt1 * 32 + sl1, 1u, __ATOMIC_RELAXED, __HIP_MEMORY_SCOPE_AGENT);
        const gu32* c = fm + (mt >> 1) * 32 + (lane & 15);
        for (int spin = 0; spin < (1 << 21); ++spin) {
            if (__all(__hip_atomic_load(c, __ATOMIC_RELAXED, __HIP_MEMORY_SCOPE_AGENT) != 0u)) break;
            __builtin_amdgcn_s_sleep(POLL_SLEEP2);
        }
    }
    asm volatile("s_waitcnt vmcnt(0)\n\ts_barrier" ::: "memory");
#pragma unroll
    for (int s = 0; s < 7; ++s) issue_h();
    asm volatile("s_waitcnt vmcnt(%0)\n\ts_barrier" :: "n"(12) : "memory");
    issue_w(); issue_h();
#define K4_BAR_ISSUE(n) asm volatile("s_waitcnt vmcnt(%0)\n\ts_barrier" :: "n"(n) : "memory"); issue_w(); issue_h()
    K4_BAR_ISSUE(2 * 5 + 1 * F);
    K4_BAR_ISSUE(2 * 4 + 2 * F);
    K4_BAR_ISSUE(2 * 3 + 3 * F);
    K4_BAR_ISSUE(2 * 2 + 4 * F);
    K4_BAR_ISSUE(2 * 1 + 5 * F);
    K4_BAR_ISSUE(6 * F);
#pragma unroll 1
    for (int st = 7; st <= 19; ++st) { K4_BAR_ISSUE(6 * F); }
#undef K4_BAR_ISSUE
    asm volatile("s_waitcnt vmcnt(%0)\n\ts_barrier" :: "n"(6 * F) : "memory");
    asm volatile("s_waitcnt vmcnt(%0)\n\ts_barrier" :: "n"(5 * F) : "memory");
    asm volatile("s_waitcnt vmcnt(%0)\n\ts_barrier" :: "n"(4 * F) : "memory");
    asm volatile("s_waitcnt vmcnt(%0)\n\ts_barrier" :: "n"(3 * F) : "memory");
    asm volatile("s_waitcnt vmcnt(%0)\n\ts_barrier" :: "n"(2 * F) : "memory");
    asm volatile("s_waitcnt vmcnt(%0)\n\ts_barrier" :: "n"(1 * F) : "memory");
    asm volatile("s_waitcnt vmcnt(0)\n\ts_barrier" ::: "memory");
    asm volatile("s_barrier" ::: "memory");
    asm volatile("s_barrier" ::: "memory");
}

__device__ __forceinline__ void k4_consumer(char* __restrict__ ws, char* smem, unsigned short* __restrict__ out, int mt, int nt, int wave, int lane STAMP_ARG) {
    const int th = wave & 1, kh = wave >> 1;
    const int tok = lane & 15, g = lane >> 4;
    const int boff = 2 * th * 2048 + kh * 1024 + lane * 16;
    const int aoff = 8192 + kh * 5120 + lane * 16;
    f32x4 acc[2][5];
#pragma unroll
    for (int t = 0; t < 2; ++t)
#pragma unroll
        for (int D = 0; D < 5; ++D) acc[t][D] = f32x4{0.f, 0.f, 0.f, 0.f};
    struct Fr { b16x8 a[5], b[2]; };
    auto load = [&](Fr& f, const char* sl) {
#pragma unroll
        for (int t = 0; t < 2; ++t) f.b[t] = *(const b16x8*)(sl + boff + t * 2048);
#pragma unroll
        for (int D = 0; D < 5; ++D) f.a[D] = *(const b16x8*)(sl + aoff + D * 1024);
    };
    auto step = [&](const Fr& c, Fr& n, const char* sl) {
#pragma unroll
        for (int D = 0; D < 5; ++D)
#pragma unroll
            for (int t = 0; t < 2; ++t) {
                acc[t][D] = __builtin_amdgcn_mfma_f32_16x16x32_bf16(c.a[D], c.b[t], acc[t][D], 0, 0, 0);
                const int m = 2 * D + t;
                if (m < 2) n.b[m] = *(const b16x8*)(sl + boff + m * 2048);
                else if (m < 7) n.a[m - 2] = *(const b16x8*)(sl + aoff + (m - 2) * 1024);
                GAP();
            }
    };
    Fr f0, f1;
    asm volatile("s_barrier" ::: "memory");
    DEADLINE(3);
    asm volatile("s_barrier" ::: "memory");
    STAMP(6);
    load(f0, smem);
    int slot = K4_STAGE;
#pragma unroll 1
    for (int st = 1; st < 27; st += 2) {
        asm volatile("s_waitcnt lgkmcnt(0)\n\ts_barrier" ::: "memory");
        GAP();
        step(f0, f1, smem + slot);
        slot = (slot == (K4_NS - 1) * K4_STAGE) ? 0 : slot + K4_STAGE;
        asm volatile("s_waitcnt lgkmcnt(0)\n\ts_barrier" ::: "memory");
        GAP();
        step(f1, f0, smem + slot);
        slot = (slot == (K4_NS - 1) * K4_STAGE) ? 0 : slot + K4_STAGE;
    }
#pragma unroll
    for (int D = 0; D < 5; ++D)
#pragma unroll
        for (int t = 0; t < 2; ++t) acc[t][D] = __builtin_amdgcn_mfma_f32_16x16x32_bf16(f0.a[D], f0.b[t], acc[t][D], 0, 0, 0);
    asm volatile("" :: "v"(acc[1][4][0])); STAMP(7);
    asm volatile("s_waitcnt lgkmcnt(0)\n\ts_barrier" ::: "memory");
    f32x4* red = (f32x4*)smem;
#pragma unroll
    for (int D = 0; D < 5; ++D) red[((th * 2 + kh) * 5 + D) * 64 + lane] = kh ? acc[0][D] : acc[1][D];
    asm volatile("s_waitcnt lgkmcnt(0)\n\ts_barrier" ::: "memory");
    const int token = 64 * mt + 32 * th + 16 * kh + tok;
    const int q = (lane & 15) >> 2, p = lane & 3;
#pragma unroll
    for (int D = 0; D < 5; ++D) {
        const f32x4 o = red[((th * 2 + (kh ^ 1)) * 5 + D) * 64 + lane];
        const f32x4 m = kh ? acc[1][D] : acc[0][D];
        const s16x4 hr = tr_read(smem + K4_HOFF + ((4 * D + g) * 4 + 2 * th + kh) * 128 + q * 32 + 8 * p);
        const u32x2 hw = bc<u32x2>(hr);
        const float r0 = lo16(hw[0]) + bf16r(m[0] + o[0]), r1 = hi16(hw[0]) + bf16r(m[1] + o[1]);
        const float r2 = lo16(hw[1]) + bf16r(m[2] + o[2]), r3 = hi16(hw[1]) + bf16r(m[3] + o[3]);
        *(u32x2*)(out + (size_t)token * DM + 80 * nt + 16 * D + 4 * g) = u32x2{pk_bf16(r0, r1), pk_bf16(r2, r3)};
    }
    STAMP(8);
    STAMP(9);
}

constexpr int KB_LDS = K4_LDS > K3_LDS ? K4_LDS : K3_LDS;
static_assert(KB_LDS >= K2_LDS && KB_LDS <= 163840, "LDS budget");

template <int NT>
__device__ __forceinline__ void ffn_block(char* __restrict__ ws, char* smem, unsigned short* __restrict__ out, int mt, int sl, int T0,
                                          int mt2, int nt, int wave, int lane STAMP_ARG) {
    if (wave < 4) {
        asm volatile("s_barrier" ::: "memory");
        DEADLINE(2);
        STAMP(15);
        k3_consumer<NT>(ws, smem, mt, T0, wave, lane STAMP_PASS);
        k4_consumer(ws, smem, out, mt2, nt, wave, lane STAMP_PASS);
    } else {
        if (wave == 4) {
            const __amdgpu_buffer_rsrc_t srd = make_srd(ws, (u32)WS_END);
            const u32 off = (u32)OFF_FG + (blockIdx.x & 15) * 1024 + lane * 16;
            for (int spin = 0; spin < (1 << 21); ++spin) {
                const u32x4 f = __builtin_amdgcn_raw_buffer_load_b128(srd, off, 0, 16);
                const bool ok = (lane == 60) ? (f[0] != 0u) : (lane > 60) || ((f[0] & f[1] & f[2] & f[3]) != 0u);
                if (__all(ok)) break;
                __builtin_amdgcn_s_sleep(POLL_SLEEP);
                asm volatile("" ::: "memory");
            }
        }
        asm volatile("s_waitcnt vmcnt(0)\n\ts_barrier" ::: "memory");
        k3_loader<NT>(ws, smem, mt, sl, T0, wave - 4, lane);
        if (wave < 6) k4_loader<3>(ws, smem, mt, sl, mt2, nt, wave - 4, lane);
        else k4_loader<2>(ws, smem, mt, sl, mt2, nt, wave - 4, lane);
    }
}

__global__ __launch_bounds__(512, 2) void k2_fused(const unsigned short* __restrict__ Dskip, const char* __restrict__ w1,
                                                   const char* __restrict__ w3, const char* __restrict__ w2, char* __restrict__ ws,
                                                   unsigned short* __restrict__ out) {
    extern __shared__ __attribute__((aligned(16))) char smem[];
    const int tid = threadIdx.x, wave = rfl(tid >> 6), lane = tid & 63;
    const int b = blockIdx.x;
    STAMP_DECL; STAMP(0);
    DEADLINE(1);
    if (b < 160) conv_block(b, Dskip, ws, smem STAMP_PASS);
    else if (b < 214) prep13_block(b - 160, w1, w3, ws, smem);
    else if (b < 241) prep2_block(b - 214, w2, ws, smem);
    STAMP(13);
    asm volatile("s_waitcnt vmcnt(0) lgkmcnt(0)\n\ts_barrier" ::: "memory");
    STAMP(14);
    if (tid >= 256 && tid < 272 && b < 241)
        __hip_atomic_store((gu32*)(ws + OFF_FG) + (tid - 256) * 256 + b, 1u, __ATOMIC_RELAXED, __HIP_MEMORY_SCOPE_AGENT);
    const int xcd = b & 7, jb = b >> 3;
    const int mt = 8 * (xcd & 1) + (jb & 7), sl = 4 * (xcd >> 1) + (jb >> 3);
    const int mt2 = 8 * (xcd >> 1) + (jb & 7), nt = 4 * (xcd & 1) + (jb >> 3);
    if (sl < 12) ffn_block<7>(ws, smem, out, mt, sl, 14 * sl, mt2, nt, wave, lane STAMP_PASS);
    else ffn_block<6>(ws, smem, out, mt, sl, 168 + 12 * (sl - 12), mt2, nt, wave, lane STAMP_PASS);
    STAMP_FLUSH(ws, 1);
}

}

extern "C" void kernel_launch(void* const* d_in, const int* in_sizes, int n_in, void* d_out, int out_size, void* d_ws,
                              size_t ws_size, hipStream_t stream) {
    (void)in_sizes; (void)n_in; (void)out_size; (void)ws_size;
    char* ws = (char*)d_ws;
    hipFuncSetAttribute((const void*)k1_ln_kgen, hipFuncAttributeMaxDynamicSharedMemorySize, K1_LDS);
    hipFuncSetAttribute((const void*)k2_fused, hipFuncAttributeMaxDynamicSharedMemorySize, KB_LDS);
    k1_ln_kgen<<<128 + 320 + WARM_BLOCKS, 256, K1_LDS, stream>>>((const u32x4*)d_in[0], (const unsigned short*)d_in[1], (const unsigned short*)d_in[2],
                                                                 (const unsigned short*)d_in[3], (const unsigned short*)d_in[4],
                                                                 (const unsigned short*)d_in[5], ws, (const char*)d_in[8], (const char*)d_in[9],
                                                                 (const char*)d_in[10], (const char*)d_out);
    k2_fused<<<256, 512, KB_LDS, stream>>>((const unsigned short*)d_in[6], (const char*)d_in[8], (const char*)d_in[9],
                                           (const char*)d_in[10], ws, (unsigned short*)d_out);
}
```

```cpp
#include <hip/hip_runtime.h>
#include <hip/hip_bf16.h>
#include <stdint.h>

#pragma clang diagnostic ignored "-Winline-asm"
#pragma clang diagnostic ignored "-Wunused-value"

namespace {

constexpr int SEQ = 2048, DM = 640, NMODE = 128, FF = 1728;

typedef _Float16 f16;
typedef f16 f16x2 __attribute__((ext_vector_type(2)));
typedef f16 f16x4 __attribute__((ext_vector_type(4)));
typedef f16 f16x8 __attribute__((ext_vector_type(8)));
typedef __bf16 b16;
typedef b16 b16x2 __attribute__((ext_vector_type(2)));
typedef b16 b16x4 __attribute__((ext_vector_type(4)));
typedef b16 b16x8 __attribute__((ext_vector_type(8)));
typedef short s16x4 __attribute__((ext_vector_type(4)));
typedef float f32x2 __attribute__((ext_vector_type(2)));
typedef float f32x4 __attribute__((ext_vector_type(4)));
typedef float f32x16 __attribute__((ext_vector_type(16)));
typedef unsigned u32;
typedef u32 u32x2 __attribute__((ext_vector_type(2)));
typedef u32 u32x4 __attribute__((ext_vector_type(4)));
#define LDS_AS __attribute__((address_space(3)))
typedef __attribute__((address_space(1))) unsigned long long gu64;
typedef __attribute__((address_space(1))) unsigned gu32;

constexpr size_t SZ_T    = (size_t)SEQ * DM * 2;
constexpr size_t OFF_ST1 = 0;
constexpr size_t OFF_HT  = OFF_ST1 + SEQ * 8;
constexpr size_t OFF_W13 = OFF_HT + SZ_T;
constexpr size_t OFF_W2  = OFF_W13 + (size_t)108 * 40 * 1024;
constexpr size_t OFF_HM  = OFF_W2 + (size_t)8 * 54 * 5 * 1024;
constexpr size_t OFF_CS  = OFF_HM + (size_t)64 * 108 * 1024;
constexpr size_t OFF_SG  = OFF_CS + (size_t)216 * 16 * 4;
constexpr size_t OFF_F1  = OFF_SG + 16 * 256 * 16;
constexpr size_t OFF_F2  = OFF_F1 + 16 * 256;
constexpr size_t OFF_FM  = OFF_F2 + 16 * 1024;
constexpr size_t OFF_EP  = OFF_FM + 16 * 128;
constexpr size_t WS_END  = OFF_EP + 128;
constexpr u32 TAG_STEP   = 0x9E3779B9u;

#define STAMP_DECL do {} while (0)
#define STAMP_ARG
#define STAMP_PASS
#define STAMP(i) do {} while (0)
#define STAMP_FLUSH(ws, k) do {} while (0)

template <class T, class S> __device__ __forceinline__ T bc(S s) { return __builtin_bit_cast(T, s); }
__device__ __forceinline__ float lo16(u32 w) { return bc<float>(w << 16); }
__device__ __forceinline__ float hi16(u32 w) { return bc<float>(w & 0xffff0000u); }
__device__ __forceinline__ float bf2f(unsigned short u) { return bc<float>((u32)u << 16); }
__device__ __forceinline__ u32 pk_bf16(float a, float b) { f32x2 v = {a, b}; return bc<u32>(__builtin_convertvector(v, b16x2)); }
__device__ __forceinline__ u32 pk_f16(float a, float b) { f32x2 v = {a, b}; return bc<u32>(__builtin_convertvector(v, f16x2)); }
__device__ __forceinline__ float bf16r(float a) { return lo16(pk_bf16(a, a)); }
__device__ __forceinline__ int rfl(int v) { return __builtin_amdgcn_readfirstlane(v); }

template <int CTRL> __device__ __forceinline__ float dpp_add(float v) {
    return v + bc<float>(__builtin_amdgcn_update_dpp(0, bc<int>(v), CTRL, 0xf, 0xf, true));
}
__device__ __forceinline__ float row16_sum(float v) {
    v = dpp_add<0xB1>(v);
    v = dpp_add<0x4E>(v);
    v = dpp_add<0x141>(v);
    v = dpp_add<0x140>(v);
    return v;
}
__device__ __forceinline__ float wave_sum(float v) {
    v = row16_sum(v);
    v += __shfl_xor(v, 16);
    v += __shfl_xor(v, 32);
    return v;
}
__device__ __forceinline__ s16x4 tr_read(const char* p) {
    return __builtin_amdgcn_ds_read_tr16_b64_v4i16((LDS_AS s16x4*)p);
}
template <class T> __device__ __forceinline__ T cat8(s16x4 a, s16x4 b) {
    return bc<T>(__builtin_shufflevector(a, b, 0, 1, 2, 3, 4, 5, 6, 7));
}
__device__ __forceinline__ void dma16s_sc1(u32 slot, u32 off, u32 voff, __amdgpu_buffer_rsrc_t srd, u32 soff) {
    asm volatile("s_add_u32 m0, %0, %1\n\ts_nop 0\n\tbuffer_load_dwordx4 %2, %3, %4 offen sc1 lds"
                 :: "s"(slot), "s"(off), "v"(voff), "s"(srd), "s"(soff) : "m0", "scc", "memory");
}
__device__ __forceinline__ __amdgpu_buffer_rsrc_t make_srd(const void* p, u32 bytes) {
    return __builtin_amdgcn_make_buffer_rsrc((void*)p, (short)0, (int)bytes, 0x00020000);
}
#define ST_AUX 16
__device__ __forceinline__ void st16(__amdgpu_buffer_rsrc_t srd, u32 off, u32x4 v) { __builtin_amdgcn_raw_buffer_store_b128(v, srd, off, 0, ST_AUX); }
__device__ __forceinline__ void st8(__amdgpu_buffer_rsrc_t srd, u32 off, u32x2 v) { __builtin_amdgcn_raw_buffer_store_b64(v, srd, off, 0, ST_AUX); }
__device__ __forceinline__ void dma16(u32 lds, u32 voff, __amdgpu_buffer_rsrc_t srd, u32 soff) {
    asm volatile("s_mov_b32 m0, %0\n\ts_nop 0\n\tbuffer_load_dwordx4 %1, %2, %3 offen lds"
                 :: "s"(lds), "v"(voff), "s"(srd), "s"(soff) : "m0", "memory");
}
__device__ __forceinline__ void dma16s(u32 slot, u32 off, u32 voff, __amdgpu_buffer_rsrc_t srd, u32 soff) {
    asm volatile("s_add_u32 m0, %0, %1\n\ts_nop 0\n\tbuffer_load_dwordx4 %2, %3, %4 offen lds"
                 :: "s"(slot), "s"(off), "v"(voff), "s"(srd), "s"(soff) : "m0", "scc", "memory");
}
__device__ __forceinline__ u32x4 ld16_sc1(__amdgpu_buffer_rsrc_t srd, u32 off) { return __builtin_amdgcn_raw_buffer_load_b128(srd, off, 0, 16); }
__device__ __forceinline__ u32x4 poll16(__amdgpu_buffer_rsrc_t srd, u32 off) {
    u32x4 r;
    asm volatile("buffer_load_dwordx4 %0, %1, %2, 0 offen sc1\n\ts_waitcnt vmcnt(0)" : "=&v"(r) : "v"(off), "s"(srd) : "memory");
    return r;
}
#define GAP() __builtin_amdgcn_sched_barrier(0)
#define WAIT_VM_LGKM_BAR(n) asm volatile("s_waitcnt vmcnt(" #n ") lgkmcnt(0)\n\ts_barrier" ::: "memory")

constexpr int KD_BYTES = 1536;
struct __attribute__((aligned(16))) IdxTab { unsigned short v[SEQ + 8]; };
constexpr int bf16pos(int l) {
    if (l < 256) return l;
    const int sh = (l < 512) ? 1 : (l < 1024) ? 2 : 3;
    int q = l >> sh; const int rem = l & ((1 << sh) - 1), half = 1 << (sh - 1);
    if (rem > half || (rem == half && (q & 1))) ++q;
    return q << sh;
}
constexpr IdxTab make_idx() {
    IdxTab t{};
    for (int l = -4; l < SEQ + 4; ++l) {
        if (l < 0 || l >= SEQ) { t.v[l + 4] = (unsigned short)KD_BYTES; continue; }
        const int p = bf16pos(l);
        int row = 0, b = 0;
        if (p < 256) { row = p & 7; b = p >> 3; }
        else if (p < 512) { const int o = p - 256; row = 8 + ((o & 7) >> 1); b = o >> 3; }
        else if (p < 1024) { const int o = p - 512, o2 = o & 255; row = 12 + 2 * (o >> 8) + ((o2 & 7) >> 2); b = o2 >> 3; }
        else if (p < 2048) { const int o = p - 1024; row = 16 + (o >> 8); b = (o & 255) >> 3; }
        else { row = 20; b = 0; }
        t.v[l + 4] = (unsigned short)((row * 32 + b) * 2);
    }
    return t;
}
__device__ const IdxTab IDX_TAB = make_idx();

#define POLL_SLEEP 8
#define POLL_SLEEP1 4
#define POLL_SLEEP2 4
#define PREP_DELAY 1

__device__ __forceinline__ void ln1_block(int j32, const u32x4* __restrict__ x, char* __restrict__ ws, u32 ev STAMP_ARG) {
    const int tid = threadIdx.x, r = tid >> 4, c = tid & 15;
    const u32x4* row = x + (size_t)(32 * j32 + r) * 80;
    u32x4 v[5];
#pragma unroll
    for (int j = 0; j < 5; ++j) v[j] = row[j * 16 + c];
    float s = 0.f, ss = 0.f, s_b = 0.f, ss_b = 0.f;
#pragma unroll
    for (int j = 0; j < 5; ++j)
#pragma unroll
        for (int e = 0; e < 4; ++e) {
            const float lo = lo16(v[j][e]), hi = hi16(v[j][e]);
            s += lo; ss = fmaf(lo, lo, ss); s_b += hi; ss_b = fmaf(hi, hi, ss_b);
        }
    const float mean = row16_sum(s + s_b) * (1.0f / DM);
    const float var = fmaxf(row16_sum(ss + ss_b) * (1.0f / DM) - mean * mean, 0.f);
    const float rinv = __builtin_amdgcn_rcpf(bf16r(__builtin_amdgcn_sqrtf(bf16r(bf16r(var) + 1e-5f))));
    const __amdgpu_buffer_rsrc_t srd = make_srd(ws, (u32)WS_END);
    if (c == 0) st8(srd, (u32)OFF_ST1 + (u32)((32 * j32 + r) * 8), u32x2{bc<u32>(bf16r(mean)), bc<u32>(rinv)});
    STAMP(1);
    asm volatile("s_waitcnt vmcnt(0)\n\ts_barrier" ::: "memory");
    if (tid < 16) __hip_atomic_store((gu32*)(ws + OFF_F1) + tid * 64 + j32, (u32)rfl((int)ev) + TAG_STEP, __ATOMIC_RELAXED, __HIP_MEMORY_SCOPE_AGENT);
    STAMP(3);
}

typedef f32x2 cplx;
struct cmulw { f32x2 rr, ii; };
__device__ __forceinline__ cmulw prepw(cplx w) { return {f32x2{w[0], w[0]}, f32x2{-w[1], w[1]}}; }
__device__ __forceinline__ cplx cmul(cplx a, cmulw w) { return __builtin_elementwise_fma(f32x2{a[1], a[0]}, w.ii, a * w.rr); }
__device__ __forceinline__ cplx csqr(cplx a) { return cmul(a, prepw(a)); }
__device__ __forceinline__ cplx cexp_p(float a2, float th, float tl, float p) {
    const float mag = __builtin_amdgcn_exp2f(a2 * p);
    const float rev = __builtin_amdgcn_fractf(th * p) + tl * p;
    return cplx{mag * __builtin_amdgcn_cosf(rev), mag * __builtin_amdgcn_sinf(rev)};
}

constexpr int KG_PITCH = 272;
constexpr int KG_WAVE  = 53 * KG_PITCH;
constexpr int K2_KCOPY = 4160;
constexpr int K2_KCH   = 4 * K2_KCOPY;
constexpr int K2_UCH   = 4640;
constexpr int K2_UOFF  = 4 * K2_KCH;
constexpr int K2_XOFF  = K2_UOFF + 4 * K2_UCH;
constexpr int K2_KD    = 8 * KG_WAVE;
constexpr int K2_LDS   = K2_KD + 4 * 1552 + 64;
static_assert(K2_KD >= 8 * KG_WAVE, "compact tables must not overlay the power tables");

__device__ __forceinline__ void conv_block(int d4, const char* __restrict__ x, const unsigned short* __restrict__ logA, const unsigned short* __restrict__ Aim,
                                           const unsigned short* __restrict__ Cre, const unsigned short* __restrict__ Cim,
                                           const unsigned short* __restrict__ logdt, const unsigned short* __restrict__ Dskip,
                                           char* __restrict__ ws, char* smem, u32 ev STAMP_ARG) {
    const int tid = threadIdx.x, wave = rfl(tid >> 6), lane = tid & 63;
    const int dch = wave & 3, hf = wave >> 2;
    const int d = 4 * d4 + dch;
    const int n = lane & 15, g = lane >> 4;
    char* Kd = smem + K2_KD + dch * 1552;
    float* ksc = (float*)(smem + K2_KD + 4 * 1552);
    u32x4 ixa[2]; u32x2 ixb[2], xr[2][2];
    const __amdgpu_buffer_rsrc_t srd = make_srd(ws, (u32)WS_END);
    {
        const int mo = d * NMODE + hf * 64 + lane;
        const float la = bf2f(logA[mo]), ai = bf2f(Aim[mo]), cr0 = bf2f(Cre[mo]), ci0 = bf2f(Cim[mo]);
        const float dt = bf16r(expf(bf2f(logdt[d])));
        asm volatile("" :: "v"(la), "v"(ai), "v"(cr0), "v"(ci0), "v"(dt));
        {
            const u32 xo = (u32)(2 * tid * (DM * 2) + d4 * 8);
            const __amdgpu_buffer_rsrc_t sx = make_srd(x, SEQ * DM * 2);
            asm volatile("buffer_load_dwordx2 %0, %4, %6, 0 offen\n\tbuffer_load_dwordx2 %1, %4, %6, 0 offen offset:1280\n\t"
                         "buffer_load_dwordx2 %2, %5, %6, 0 offen\n\tbuffer_load_dwordx2 %3, %5, %6, 0 offen offset:1280"
                         : "=&v"(xr[0][0]), "=&v"(xr[0][1]), "=&v"(xr[1][0]), "=&v"(xr[1][1])
                         : "v"(xo), "v"(xo + 1024u * (DM * 2)), "s"(sx) : "memory");
        }
#pragma unroll
        for (int j = 0; j < 2; ++j) {
            const int m = lane + 64 * (2 * hf + j);
            ixa[j] = *(const u32x4*)(IDX_TAB.v + 8 * m); ixb[j] = *(const u32x2*)(IDX_TAB.v + 8 * m + 8);
        }
        const float zr = -bf16r(expf(la)) * dt, zi = ai * dt;
        const float cr = cr0 * dt, ci = ci0 * dt;
        const float Bw = wave_sum(fabsf(cr) + fabsf(ci));
        int ew = __builtin_amdgcn_frexp_expf(Bw); ew = min(max(ew, -100), 100);
        const float Sw = ldexpf(1.0f, 14 - ew), iSw = ldexpf(1.0f, ew - 14);
        const float a2 = zr * 1.44269504088896341f;
        const float INV2PI_HI = 0.15915493667125702f, INV2PI_LO = 6.4206382520e-09f;
        const float th = zi * INV2PI_HI;
        const float tl = fmaf(zi, INV2PI_HI, -th) + zi * INV2PI_LO;
        const cplx w1 = cexp_p(a2, th, tl, 1.f), w8 = cexp_p(a2, th, tl, 8.f), w256 = cexp_p(a2, th, tl, 256.f);
        const cplx w2 = csqr(w1), w4 = csqr(w2), w16 = csqr(w8);
        const cplx w512 = csqr(w256), w1024 = csqr(w512), w2048 = csqr(w1024);
        char* Hs = smem + wave * KG_WAVE;
        char* Es = Hs + 21 * KG_PITCH;
        auto putH = [&](int row, cplx v) { *(u32*)(Hs + row * KG_PITCH + lane * 4) = pk_f16(v[0], -v[1]); };
        auto putE = [&](int row, cplx v) { *(u32*)(Es + row * KG_PITCH + lane * 4) = pk_f16(v[0], v[1]); };
        const cplx c0 = {cr * Sw, ci * Sw};
        const cmulw m1 = prepw(w1), m2 = prepw(w2), m4 = prepw(w4), m16 = prepw(w16), m256 = prepw(w256);
        cplx e0 = {1.f, 0.f}, e1 = w8;
#pragma unroll
        for (int b = 0; b < 32; b += 2) { putE(b, e0); putE(b + 1, e1); if (b < 30) { e0 = cmul(e0, m16); e1 = cmul(e1, m16); } }
        cplx h = c0;
#pragma unroll
        for (int k = 0; k < 8; ++k) { putH(k, h); if (k < 7) h = cmul(h, m1); }
        h = cmul(c0, m256);
#pragma unroll
        for (int k = 0; k < 4; ++k) { putH(8 + k, h); if (k < 3) h = cmul(h, m2); }
        const cplx c512 = cmul(c0, prepw(w512)), c768 = cmul(c512, m256);
        putH(12, c512); putH(13, cmul(c512, m4)); putH(14, c768); putH(15, cmul(c768, m4));
        h = cmul(c0, prepw(w1024));
#pragma unroll
        for (int k = 0; k < 4; ++k) { putH(16 + k, h); if (k < 3) h = cmul(h, m256); }
        putH(20, cmul(c0, prepw(w2048)));
        STAMP(1);
        const int r = lane & 31, hh = lane >> 5;
        const char* ap = Hs + min(r, 20) * KG_PITCH + 16 * hh;
        const char* bp = Es + r * KG_PITCH + 16 * hh;
        f16x8 fa[8], fb[8];
#pragma unroll
        for (int s = 0; s < 8; ++s) { fa[s] = *(const f16x8*)(ap + 32 * s); fb[s] = *(const f16x8*)(bp + 32 * s); }
        GAP();
        f32x16 acc = {};
#pragma unroll
        for (int s = 0; s < 8; ++s) acc = __builtin_amdgcn_mfma_f32_32x32x16_f16(fa[s], fb[s], acc, 0, 0, 0);
        float* mine = (float*)(smem + wave * KG_WAVE);
        const float* other = (const float*)(smem + (wave ^ 4) * KG_WAVE);
#pragma unroll
        for (int i = 0; i < 6; ++i) mine[i * 64 + lane] = (hf ? acc[i] : acc[6 + i]) * iSw;
        if (lane == 0) ksc[4 + wave] = Bw;
        asm volatile("s_waitcnt lgkmcnt(0)\n\ts_barrier" ::: "memory");
        const float Bt = 2.0f * (ksc[4 + dch] + ksc[8 + dch]);
        int eo = __builtin_amdgcn_frexp_expf(Bt); eo = min(max(eo, -100), 100);
        const float So = ldexpf(2.0f, 14 - eo);
#pragma unroll
        for (int k = 0; k < 6; ++k) {
            const int i = hf ? 6 + k : k;
            const float kv = ((hf ? acc[6 + k] : acc[k]) * iSw + other[k * 64 + lane]) * So;
            *(f16*)(Kd + (((i & 3) + 8 * (i >> 2) + 4 * hh) * 32 + r) * 2) = (f16)kv;
        }
        if (!hf && lane == 0) { ksc[dch] = ldexpf(1.0f, eo - 14); *(u32*)(Kd + KD_BYTES) = 0u; }
        if (wave == 4) {
            const u32 tag = (u32)rfl((int)ev) + TAG_STEP;
            const u32 off = (u32)OFF_F1 + (blockIdx.x & 15) * 256 + (lane & 15) * 16;
            for (int spin = 0; spin < (1 << 21); ++spin) {
                const u32x4 f = poll16(srd, off);
                if (__all(f[0] == tag && f[1] == tag && f[2] == tag && f[3] == tag)) break;
                __builtin_amdgcn_s_sleep(POLL_SLEEP1);
            }
        }
        asm volatile("s_waitcnt lgkmcnt(0)\n\ts_barrier" ::: "memory");
    }
    STAMP(2);
    u32x4 s1c[2];
#pragma unroll
    for (int j = 0; j < 2; ++j) s1c[j] = ld16_sc1(srd, (u32)OFF_ST1 + (u32)((tid + 512 * j) * 16));
    char* Kb = smem + dch * K2_KCH;
    char* Ub = smem + K2_UOFF;
    if (hf == 0 && lane < 8) *(u32x4*)(Kb + (lane >> 1) * K2_KCOPY + (lane & 1) * 16) = u32x4{0u, 0u, 0u, 0u};
    if (tid < 128) *(u32x4*)(Ub + (tid >> 5) * K2_UCH + (tid & 31) * 16) = u32x4{0u, 0u, 0u, 0u};
#pragma unroll
    for (int j = 0; j < 2; ++j) {
        const u32 ix[6] = {ixa[j][0], ixa[j][1], ixa[j][2], ixa[j][3], ixb[j][0], ixb[j][1]};
        u32 w[6];
#pragma unroll
        for (int e = 0; e < 6; ++e) {
            const u32 lo = *(const unsigned short*)(Kd + (ix[e] & 0xffffu)), hi = *(const unsigned short*)(Kd + (ix[e] >> 16));
            w[e] = lo | (hi << 16);
        }
        u32 a[5];
#pragma unroll
        for (int e = 0; e < 5; ++e) a[e] = __builtin_amdgcn_alignbit(w[e + 1], w[e], 16);
        char* o = Kb + 32 + (lane + 64 * (2 * hf + j)) * 16;
        *(u32x4*)(o)                = u32x4{w[2], w[3], w[4], w[5]};
        *(u32x4*)(o + K2_KCOPY)     = u32x4{a[1], a[2], a[3], a[4]};
        *(u32x4*)(o + 2 * K2_KCOPY) = u32x4{w[1], w[2], w[3], w[4]};
        *(u32x4*)(o + 3 * K2_KCOPY) = u32x4{a[0], a[1], a[2], a[3]};
    }
    const float invS = ksc[dch];
    const float dsk = bf2f(Dskip[d]);
    STAMP(3);
    asm volatile("s_waitcnt vmcnt(0)" : "+v"(xr[0][0]), "+v"(xr[0][1]), "+v"(xr[1][0]), "+v"(xr[1][1]), "+v"(s1c[0]), "+v"(s1c[1]) :: "memory");
#pragma unroll
    for (int j = 0; j < 2; ++j) {
        const int P = tid + 512 * j;
        const f32x4 c2 = bc<f32x4>(s1c[j]);
#pragma unroll
        for (int e = 0; e < 2; ++e) {
            const u32 a = xr[j][0][e], b = xr[j][1][e];
            const u32 xl = __builtin_amdgcn_perm(b, a, 0x05040100u), xh = __builtin_amdgcn_perm(b, a, 0x07060302u);
#pragma unroll
            for (int h = 0; h < 2; ++h) {
                const u32 xx = h ? xh : xl;
                const u32 dd = pk_bf16(lo16(xx) - c2[0], hi16(xx) - c2[2]);
                const u32 qq = pk_bf16(lo16(dd) * c2[1], hi16(dd) * c2[3]);
                *(u32*)(Ub + (2 * e + h) * K2_UCH + 512 + 4 * P) = pk_f16(lo16(qq), hi16(qq));
                *(u32*)(smem + K2_XOFF + (2 * e + h) * 4096 + 4 * P) = xx;
            }
        }
    }
    __syncthreads();
    STAMP(4);
    const int dd = g >> 1, h1 = g & 1, q4 = n >> 2, p = n & 3;
    const char* kb = Kb + q4 * K2_KCOPY + 2 * (16 + 16 * dd - 8 * h1 + 4 * p) - 8 + 256 * hf;
    const char* ub = Ub + dch * K2_UCH + 512 + 32 * (n - dd) + 16 * h1 - 64 * 3 - 256 * hf;
    f32x4 acc[8];
#pragma unroll
    for (int q = 0; q < 8; ++q) acc[q] = f32x4{0.f, 0.f, 0.f, 0.f};
    struct Fr { f16x8 a[8], b[8]; };
    auto load = [&](Fr& f, int rho) {
#pragma unroll
        for (int i = 0; i < 8; ++i) {
            f.a[i] = cat8<f16x8>(tr_read(kb + 64 * rho + 512 * i + 8), tr_read(kb + 64 * rho + 512 * i));
            f.b[i] = *(const f16x8*)(ub + 512 * i + 64 * (3 - rho));
        }
    };
    auto step = [&](const Fr& c, Fr& nx, int rho, bool ld) {
        s16x4 lo[8], hi[8];
        int m = 0;
#pragma unroll
        for (int s = 0; s < 8; ++s)
#pragma unroll
            for (int i = 0; i <= s; ++i) {
                acc[s] = __builtin_amdgcn_mfma_f32_16x16x32_f16(c.a[i], c.b[s - i], acc[s], 0, 0, 0);
                if (ld && m < 24) {
                    const int k = m / 3, w = m % 3;
                    if (w == 0) nx.b[k] = *(const f16x8*)(ub + 512 * k + 64 * (3 - rho));
                    else if (w == 1) lo[k] = tr_read(kb + 64 * rho + 512 * k + 8);
                    else hi[k] = tr_read(kb + 64 * rho + 512 * k);
                }
                ++m;
                GAP();
            }
        if (ld) {
#pragma unroll
            for (int k = 0; k < 8; ++k) nx.a[k] = cat8<f16x8>(lo[k], hi[k]);
        }
    };
    Fr f0, f1;
    load(f0, 0);
    step(f0, f1, 1, true);
    step(f1, f0, 2, true);
    step(f0, f1, 3, true);
    step(f1, f0, 0, false);
    __syncthreads();
    STAMP(5);
    f32x4* red = (f32x4*)smem;
#pragma unroll
    for (int q = 0; q < 4; ++q) red[(wave * 4 + q) * 64 + lane] = hf ? acc[q] : acc[4 + q];
    __syncthreads();
    const char* ue = Ub + dch * K2_UCH + 512 + 32 * n + 8 * g + 2048 * hf;
    const char* xe0 = smem + K2_XOFF + dch * 4096 + 32 * n + 8 * g + 2048 * hf;
    char* Hst = smem + 32768;
#pragma unroll
    for (int q = 0; q < 4; ++q) {
        const f32x4 o = red[((wave ^ 4) * 4 + q) * 64 + lane];
        const f32x4 mine = hf ? acc[4 + q] : acc[q];
        const f16x4 uq = *(const f16x4*)(ue + 512 * q);
        const u32x2 xq = *(const u32x2*)(xe0 + 512 * q);
        float hv[4];
#pragma unroll
        for (int e = 0; e < 4; ++e) {
            const float yb = bf16r((mine[e] + o[e]) * invS);
            const float ud = bf16r((float)uq[e] * dsk);
            const float z = bf16r(yb + ud);
            const float xe = (e & 1) ? hi16(xq[e >> 1]) : lo16(xq[e >> 1]);
            hv[e] = xe + z;
        }
        *(u32x2*)(Hst + (16 * (4 * hf + q) + n) * 128 + dch * 32 + 8 * g) = u32x2{pk_bf16(hv[0], hv[1]), pk_bf16(hv[2], hv[3])};
    }
    __syncthreads();
#pragma unroll
    for (int j = 0; j < 2; ++j) {
        const int pc = wave + 8 * j;
        st16(srd, (u32)OFF_HT + (u32)((pc * 160 + d4) * 1024 + lane * 16), *(const u32x4*)(Hst + pc * 1024 + lane * 16));
    }
}

__device__ __forceinline__ void prep13_block(int j, const char* __restrict__ w1, const char* __restrict__ w3, char* __restrict__ ws, char* smem) {
    const int tid = threadIdx.x, wave = rfl(tid >> 6), lane = tid & 63;
    {
        u32x4 a[5], b[5];
#pragma unroll
        for (int it = 0; it < 5; ++it) {
            const int G = tid + 512 * it, k = G >> 2, c = G & 3;
            a[it] = *(const u32x4*)(w1 + (size_t)k * (FF * 2) + j * 64 + c * 16);
            b[it] = *(const u32x4*)(w3 + (size_t)k * (FF * 2) + j * 64 + c * 16);
        }
#pragma unroll
        for (int it = 0; it < 5; ++it) {
            const int G = tid + 512 * it;
            *(u32x4*)(smem + G * 32)      = u32x4{a[it][0], b[it][0], a[it][1], b[it][1]};
            *(u32x4*)(smem + G * 32 + 16) = u32x4{a[it][2], b[it][2], a[it][3], b[it][3]};
        }
    }
    __syncthreads();
    const int T = wave & 3, kh = wave >> 2;
    const int g = lane >> 4, q = (lane & 15) >> 2, p = lane & 3;
    const char* base = smem + (8 * g + q) * 128 + T * 32 + 8 * p + kh * 10 * 4096;
    float cs = 0.f;
    const b16x2 ones = {(b16)1.0f, (b16)1.0f};
    const __amdgpu_buffer_rsrc_t srd = make_srd(ws, (u32)WS_END);
#pragma unroll
    for (int ks = 0; ks < 10; ++ks) {
        const u32x4 v = cat8<u32x4>(tr_read(base + ks * 4096), tr_read(base + ks * 4096 + 512));
        st16(srd, (u32)OFF_W13 + (u32)(((4 * j + T) * 20 + 10 * kh + ks) * 1024 + lane * 16), v);
#pragma unroll
        for (int e = 0; e < 4; ++e) cs = __builtin_amdgcn_fdot2_f32_bf16(bc<b16x2>(v[e]), ones, cs, false);
    }
    cs += __shfl_xor(cs, 16); cs += __shfl_xor(cs, 32);
    float* red = (float*)(smem + 81920);
    if (lane < 16) red[wave * 16 + lane] = cs;
    __syncthreads();
    if (tid < 64) __hip_atomic_store((gu32*)(ws + OFF_CS) + 4 * j * 16 + tid, bc<u32>(red[tid] + red[64 + tid]), __ATOMIC_RELAXED, __HIP_MEMORY_SCOPE_AGENT);
}

__device__ __forceinline__ void prep2_block(int j, const char* __restrict__ w2, char* __restrict__ ws, char* smem) {
    const int tid = threadIdx.x, wave = rfl(tid >> 6), lane = tid & 63;
    const u32 lds0 = (u32)(size_t)smem;
    {
        const __amdgpu_buffer_rsrc_t s2 = make_srd(w2, DM * FF * 2);
#pragma unroll
        for (int it = 0; it < 10; ++it) {
            const int pc = wave + 8 * it, P = 64 * pc + lane, f = P / 80, c = (P % 80) ^ ((f & 7) << 1);
            dma16(lds0 + pc * 1024, f * 1280 + c * 16, s2, j * 64 * 1280);
        }
    }
    WAIT_VM_LGKM_BAR(0);
    const int g = lane >> 4, q = (lane & 15) >> 2, p = lane & 3;
    const __amdgpu_buffer_rsrc_t srd = make_srd(ws, (u32)WS_END);
#pragma unroll
    for (int sl = 0; sl < 2; ++sl)
#pragma unroll
        for (int it = 0; it < 5; ++it) {
            const int DT = wave * 5 + it;
            s16x4 r[2];
#pragma unroll
            for (int t = 0; t < 2; ++t) {
                const int f = 32 * sl + 8 * g + 4 * t + q, c = 2 * DT + (p >> 1);
                r[t] = tr_read(smem + (f * 80 + (c ^ ((f & 7) << 1))) * 16 + 8 * (p & 1));
            }
            st16(srd, (u32)OFF_W2 + (u32)((((DT / 5) * 54 + 2 * j + sl) * 5 + DT % 5) * 1024 + lane * 16), cat8<u32x4>(r[0], r[1]));
        }
}

constexpr int K3_STAGE = 16384 + 28 * 1024;
constexpr int K3_CS    = 3 * K3_STAGE;
constexpr int K3_STAT  = K3_CS + 1024;
constexpr int K3_SH    = K3_STAT + 2048;
constexpr int K3_SP    = K3_SH + 10240;
constexpr int K3_LDS   = K3_SP + 512;

struct G1Frag { b16x8 a[7]; b16x8 b[4]; };

template <int NT>
__device__ __forceinline__ void k3_consumer(char* __restrict__ ws, char* smem, int mt, int T0, int wave, int lane STAMP_ARG) {
    const int th = wave & 1, fh = wave >> 1;
    const int g = lane >> 4, q = (lane & 15) >> 2, p = lane & 3, tok = lane & 15;
    const int bo0 = 2 * g * 1024 + (4 * th + (g & 1)) * 128 + q * 32 + 8 * p;
    const int bo1 = 2 * g * 1024 + (4 * th - (g & 1)) * 128 + q * 32 + 8 * p;
#define BOFF(tt) ((((tt) & 1) ? bo1 : bo0) + 128 * (tt))
    const int aoff = 16384 + fh * NT * 2048 + lane * 16;
    f32x4 acc[4][NT];
#pragma unroll
    for (int tt = 0; tt < 4; ++tt)
#pragma unroll
        for (int i = 0; i < NT; ++i) acc[tt][i] = f32x4{0.f, 0.f, 0.f, 0.f};
    auto step = [&](const G1Frag& c, G1Frag& n, const char* sl, int ks) {
        s16x4 lo[4], hi[4];
#pragma unroll
        for (int i = 0; i < NT; ++i)
#pragma unroll
            for (int tt = 0; tt < 4; ++tt) {
                acc[tt][i] = __builtin_amdgcn_mfma_f32_16x16x32_bf16(c.a[i], c.b[tt], acc[tt][i], 0, 0, 0);
                const int m = 4 * i + tt;
                if (m >= 1 && m <= 8) {
                    const int r = m - 1;
                    if (r & 1) hi[r >> 1] = tr_read(sl + BOFF(r >> 1) + ks * 8192 + 1024);
                    else lo[r >> 1] = tr_read(sl + BOFF(r >> 1) + ks * 8192);
                } else if (m >= 9 && m < 9 + 2 * NT && ((m - 9) & 1) == 0) {
                    const int r = (m - 9) >> 1;
                    n.a[r] = *(const b16x8*)(sl + aoff + r * 2048 + ks * 1024);
                }
                GAP();
            }
#pragma unroll
        for (int tt = 0; tt < 4; ++tt) n.b[tt] = cat8<b16x8>(lo[tt], hi[tt]);
    };
    asm volatile("s_barrier" ::: "memory");
    STAMP(9);
    G1Frag f0, f1;
#pragma unroll
    for (int tt = 0; tt < 4; ++tt) f0.b[tt] = cat8<b16x8>(tr_read(smem + BOFF(tt)), tr_read(smem + BOFF(tt) + 1024));
#pragma unroll
    for (int i = 0; i < NT; ++i) f0.a[i] = *(const b16x8*)(smem + aoff + i * 2048);
    int slot = 0;
#pragma unroll 1
    for (int st = 0; st < 10; ++st) {
        step(f0, f1, smem + slot, 1);
        slot = (slot == 2 * K3_STAGE) ? 0 : slot + K3_STAGE;
        asm volatile("s_waitcnt lgkmcnt(0)\n\ts_barrier" ::: "memory");
        GAP();
        step(f1, f0, smem + slot, 0);
    }
    asm volatile("" :: "v"(acc[3][NT - 1][0])); STAMP(10);
    const __amdgpu_buffer_rsrc_t srd = make_srd(ws, (u32)WS_END);
    f32x4 cs[NT];
#pragma unroll
    for (int i = 0; i < NT; ++i) cs[i] = *(const f32x4*)(smem + K3_CS + (fh * NT + i) * 64 + g * 16);
#pragma unroll
    for (int tt = 0; tt < 4; ++tt) {
        const f32x4 st = *(const f32x4*)(smem + K3_STAT + (64 * th + 16 * tt + tok) * 16);
        const f32x2 nm2 = {st[0], st[0]}, ns2 = {st[1], st[1]}, sq2 = {st[2], st[2]};
        u32 v[8];
#pragma unroll
        for (int i = 0; i < NT; ++i) {
            const f32x2 a = __builtin_elementwise_fma(nm2, f32x2{cs[i][0], cs[i][1]}, f32x2{acc[tt][i][0], acc[tt][i][1]});
            const f32x2 b = __builtin_elementwise_fma(nm2, f32x2{cs[i][2], cs[i][3]}, f32x2{acc[tt][i][2], acc[tt][i][3]});
            const f32x2 t = a * ns2;
            const f32x2 ex = {__builtin_amdgcn_exp2f(t[0]), __builtin_amdgcn_exp2f(t[1])};
            const f32x2 dn = __builtin_elementwise_fma(ex, sq2, sq2);
            const f32x2 hm = (a * b) * f32x2{__builtin_amdgcn_rcpf(dn[0]), __builtin_amdgcn_rcpf(dn[1])};
            v[i] = bc<u32>(__builtin_convertvector(hm, b16x2));
        }
#pragma unroll
        for (int i = NT; i < 8; ++i) v[i] = 0u;
        const u32 dst = (u32)OFF_HM + (u32)(((mt * 8 + 4 * th + tt) * 216 + T0 + fh * NT + g) * 256 + tok * 16);
#pragma unroll
        for (int gr = 0; gr < 2; ++gr) {
            auto s02 = __builtin_amdgcn_permlane32_swap(v[4 * gr + 0], v[4 * gr + 2], false, false);
            auto s13 = __builtin_amdgcn_permlane32_swap(v[4 * gr + 1], v[4 * gr + 3], false, false);
            auto s01 = __builtin_amdgcn_permlane16_swap(s02[0], s13[0], false, false);
            auto s23 = __builtin_amdgcn_permlane16_swap(s02[1], s13[1], false, false);
            if (4 * gr + g < NT) st16(srd, dst + gr * 1024, u32x4{s01[0], s01[1], s23[0], s23[1]});
        }
    }
    STAMP(11);
    asm volatile("s_waitcnt vmcnt(0)\n\ts_barrier" ::: "memory");
    STAMP(12);
}

template <int NT>
__device__ __forceinline__ void k3_loader(char* __restrict__ ws, char* smem, int mt, int sl, int T0, int lw, int lane, u32 tag) {
    const u32 lds0 = (u32)(size_t)smem;
    const __amdgpu_buffer_rsrc_t srd = make_srd(ws, (u32)WS_END);
    const u32 voff = lane * 16;
    const u32 voffh0 = (lane >> 3) * 128 + (lane & 7) * 16, voffh1 = ((lane >> 3) ^ 1) * 128 + (lane & 7) * 16;
    constexpr int NW = NT;
    constexpr int NL = 4 + NW;
    u32 gh = (u32)OFF_HT + (u32)((mt * 160 + lw * 4) * 1024);
    u32 gw[NW];
#pragma unroll
    for (int i = 0; i < NW; ++i) { const int pc = lw * NW + i; gw[i] = (u32)OFF_W13 + (u32)(((T0 + (pc >> 1)) * 20 + (pc & 1)) * 1024); }
    const u32 lh = lw * 4096, lwo = 16384 + lw * NW * 1024;
    u32 islot = lds0;
    auto issue = [&]() {
#pragma unroll
        for (int i = 0; i < 4; ++i)
            dma16s_sc1(islot, lh + i * 1024, (i & 2) ? voffh1 : voffh0, srd, gh + i * 1024);
#pragma unroll
        for (int i = 0; i < NW; ++i) { dma16s_sc1(islot, lwo + i * 1024, voff, srd, gw[i]); gw[i] += 2048; }
        gh += 16384;
        islot = (islot == lds0 + 2 * K3_STAGE) ? lds0 : islot + K3_STAGE;
    };
    if (lw == 0) dma16s_sc1(lds0, K3_CS, voff, srd, (u32)OFF_CS + (u32)(T0 * 64));
    issue(); issue();
    {
        const u32 vo = (lane >> 3) * 1024 + (lane & 7) * 16;
#pragma unroll
        for (int i = 0; i < 3; ++i) {
            const int pc = min(lw + 4 * i, 9);
            dma16s_sc1(lds0, K3_SH + pc * 1024, vo, srd, (u32)OFF_HT + (u32)((mt * 160 + 80 * (sl & 1) + 8 * pc) * 1024 + (sl >> 1) * 128));
        }
    }
    asm volatile("s_waitcnt vmcnt(%0)\n\ts_barrier" :: "n"(NL + 3) : "memory");
    issue();
    asm volatile("s_waitcnt vmcnt(%0) lgkmcnt(0)\n\ts_barrier" :: "n"(NL) : "memory");
    issue();
    {
        const int g = lane >> 4, q = (lane & 15) >> 2, p = lane & 3;
        float s1 = 0.f, s2 = 0.f;
        const b16x2 ones = {(b16)1.0f, (b16)1.0f};
#pragma unroll
        for (int i = 0; i < 5; ++i) {
            const u32x2 w = bc<u32x2>(tr_read(smem + K3_SH + (20 * lw + 4 * i + g) * 128 + q * 32 + 8 * p));
#pragma unroll
            for (int e = 0; e < 2; ++e) {
                s1 = __builtin_amdgcn_fdot2_f32_bf16(bc<b16x2>(w[e]), ones, s1, false);
                s2 = __builtin_amdgcn_fdot2_f32_bf16(bc<b16x2>(w[e]), bc<b16x2>(w[e]), s2, false);
            }
        }
        s1 += __shfl_xor(s1, 16); s2 += __shfl_xor(s2, 16);
        s1 += __shfl_xor(s1, 32); s2 += __shfl_xor(s2, 32);
        if (lane < 16) *(f32x2*)(smem + K3_SP + (lw * 16 + lane) * 8) = f32x2{s1, s2};
    }
    asm volatile("s_waitcnt vmcnt(%0) lgkmcnt(0)\n\ts_barrier" :: "n"(NL) : "memory");
    if (lw == 0 && lane < 16) {
        f32x2 t = *(const f32x2*)(smem + K3_SP + lane * 8);
#pragma unroll
        for (int w = 1; w < 4; ++w) t += *(const f32x2*)(smem + K3_SP + (w * 16 + lane) * 8);
        st16(srd, (u32)OFF_SG + (u32)(((mt * 16 + sl) * 16 + lane) * 16), u32x4{bc<u32>(t[0]), tag, bc<u32>(t[1]), tag});
    }
    issue();
#pragma unroll 1
    for (int st = 2; st < 7; ++st) {
        asm volatile("s_waitcnt vmcnt(%0) lgkmcnt(0)\n\ts_barrier" :: "n"(NL) : "memory");
        issue();
    }
    if (lw < 2) {
        const int t = 64 * lw + lane;
        const char* gp = ws + OFF_SG + ((mt * 16 + 2 * (t >> 4)) * 16 + (t & 15)) * 16;
        u32x4 g0, g1;
        asm volatile("global_load_dwordx4 %0, %2, off sc1\n\tglobal_load_dwordx4 %1, %2, off offset:256 sc1" : "=&v"(g0), "=&v"(g1) : "v"(gp) : "memory");
        asm volatile("s_waitcnt vmcnt(%0) lgkmcnt(0)\n\ts_barrier" :: "n"(NL + 2) : "memory");
        asm volatile("s_waitcnt vmcnt(0) lgkmcnt(0)\n\ts_barrier" : "+v"(g0), "+v"(g1) :: "memory");
        bool ok = __all(g0[1] == tag && g0[3] == tag && g1[1] == tag && g1[3] == tag);
        for (int spin = 0; !ok && spin < 20000; ++spin) {
            __builtin_amdgcn_s_sleep(8);
            const u32 go = (u32)OFF_SG + (u32)(((mt * 16 + 2 * (t >> 4)) * 16 + (t & 15)) * 16);
            g0 = poll16(srd, go); g1 = poll16(srd, go + 256);
            ok = __all(g0[1] == tag && g0[3] == tag && g1[1] == tag && g1[3] == tag);
        }
        float s1 = bc<float>(g0[0]) + bc<float>(g1[0]);
        float s2 = bc<float>(g0[2]) + bc<float>(g1[2]);
        if (!ok) {
            s1 = 0.f; s2 = 0.f;
            const gu32* hp = (const gu32*)(ws + OFF_HT) + ((size_t)mt * 160 * 8 + (t >> 4)) * 32 + ((t & 15) >> 1);
            for (int d = 0; d < DM; ++d) {
                const u32 w = __hip_atomic_load(hp + (size_t)(d >> 2) * 256 + (d & 3) * 8, __ATOMIC_RELAXED, __HIP_MEMORY_SCOPE_AGENT);
                const float v = (t & 1) ? hi16(w) : lo16(w); s1 += v; s2 = fmaf(v, v, s2);
            }
        }
        const float mean = s1 * (1.0f / DM);
        const float var = fmaxf(s2 * (1.0f / DM) - mean * mean, 0.f);
        const float sg = bf16r(__builtin_amdgcn_sqrtf(bf16r(bf16r(var) + 1e-5f)));
        *(f32x4*)(smem + K3_STAT + t * 16) = f32x4{-bf16r(mean), -1.44269504088896341f * __builtin_amdgcn_rcpf(sg), sg * sg, 0.f};
    } else {
        asm volatile("s_waitcnt vmcnt(%0) lgkmcnt(0)\n\ts_barrier" :: "n"(NL) : "memory");
        asm volatile("s_waitcnt vmcnt(0) lgkmcnt(0)\n\ts_barrier" ::: "memory");
    }
    asm volatile("s_waitcnt lgkmcnt(0)\n\ts_barrier" ::: "memory");
}

constexpr int K4_STAGE = 18432;
constexpr int K4_NS    = 8;
constexpr int K4_HOFF  = K4_NS * K4_STAGE;
constexpr int K4_LDS   = K4_HOFF + 10240;

template <int NW>
__device__ __forceinline__ void k4_loader(char* __restrict__ ws, char* smem, int mt1, int sl1, int mt, int nt, int lw, int lane, u32 tag) {
    const u32 lds0 = (u32)(size_t)smem;
    const __amdgpu_buffer_rsrc_t srd = make_srd(ws, (u32)WS_END);
    const u32 voff = lane * 16;
    constexpr int F = NW + 2;
    const int pw0 = (lw < 2) ? lw * 3 : 6 + (lw - 2) * 2;
    u32 gh = (u32)OFF_HM + (u32)((mt * 4 + lw) * 216 * 256);
    u32 gw = (u32)OFF_W2 + (u32)((nt * 54 * 5 + pw0) * 1024);
    const u32 lh = lw * 2048, lwo = 8192 + pw0 * 1024;
    u32 hslot = lds0, wslot = lds0;
    auto next = [&](u32 sl) { return (sl == lds0 + (K4_NS - 1) * K4_STAGE) ? lds0 : sl + K4_STAGE; };
    auto issue_w = [&]() {
#pragma unroll
        for (int i = 0; i < NW; ++i) dma16s_sc1(wslot, lwo + i * 1024, voff, srd, gw + i * 1024);
        gw += 10240; wslot = next(wslot);
    };
    auto issue_h = [&]() {
#pragma unroll
        for (int i = 0; i < 2; ++i) dma16s_sc1(hslot, lh + i * 1024, voff, srd, gh + i * 1024);
        gh += 2048; hslot = next(hslot);
    };
#pragma unroll
    for (int s = 0; s < 7; ++s) issue_w();
    {
        const int d4l = lane >> 5, al = (lane >> 3) & 3, c = lane & 7;
        const u32 vo = (u32)((d4l * 8 + 4 * (mt & 1) + al) * 128 + c * 16);
#pragma unroll
        for (int i = 0; i < 3; ++i) {
            const int pc = min(lw + 4 * i, 9);
            dma16s_sc1(lds0, K4_HOFF + pc * 1024, vo, srd, (u32)OFF_HT + (u32)((((mt >> 1) * 160) + 20 * nt + 2 * pc) * 1024));
        }
    }
    asm volatile("s_barrier" ::: "memory");
    if (lw == 0) {
        gu32* fm = (gu32*)(ws + OFF_FM);
        if (lane == 0) __hip_atomic_store(fm + mt1 * 32 + sl1, tag, __ATOMIC_RELAXED, __HIP_MEMORY_SCOPE_AGENT);
        const gu32* c = fm + (mt >> 1) * 32 + (lane & 15);
        for (int spin = 0; spin < (1 << 21); ++spin) {
            if (__all(__hip_atomic_load(c, __ATOMIC_RELAXED, __HIP_MEMORY_SCOPE_AGENT) == tag)) break;
            __builtin_amdgcn_s_sleep(POLL_SLEEP2);
        }
    }
    asm volatile("s_waitcnt vmcnt(0)\n\ts_barrier" ::: "memory");
#pragma unroll
    for (int s = 0; s < 7; ++s) issue_h();
    asm volatile("s_waitcnt vmcnt(%0)\n\ts_barrier" :: "n"(12) : "memory");
    issue_w(); issue_h();
#define K4_BAR_ISSUE(n) asm volatile("s_waitcnt vmcnt(%0)\n\ts_barrier" :: "n"(n) : "memory"); issue_w(); issue_h()
    K4_BAR_ISSUE(2 * 5 + 1 * F);
    K4_BAR_ISSUE(2 * 4 + 2 * F);
    K4_BAR_ISSUE(2 * 3 + 3 * F);
    K4_BAR_ISSUE(2 * 2 + 4 * F);
    K4_BAR_ISSUE(2 * 1 + 5 * F);
    K4_BAR_ISSUE(6 * F);
#pragma unroll 1
    for (int st = 7; st <= 19; ++st) { K4_BAR_ISSUE(6 * F); }
#undef K4_BAR_ISSUE
    asm volatile("s_waitcnt vmcnt(%0)\n\ts_barrier" :: "n"(6 * F) : "memory");
    asm volatile("s_waitcnt vmcnt(%0)\n\ts_barrier" :: "n"(5 * F) : "memory");
    asm volatile("s_waitcnt vmcnt(%0)\n\ts_barrier" :: "n"(4 * F) : "memory");
    asm volatile("s_waitcnt vmcnt(%0)\n\ts_barrier" :: "n"(3 * F) : "memory");
    asm volatile("s_waitcnt vmcnt(%0)\n\ts_barrier" :: "n"(2 * F) : "memory");
    asm volatile("s_waitcnt vmcnt(%0)\n\ts_barrier" :: "n"(1 * F) : "memory");
    asm volatile("s_waitcnt vmcnt(0)\n\ts_barrier" ::: "memory");
    asm volatile("s_barrier" ::: "memory");
    asm volatile("s_barrier" ::: "memory");
}

__device__ __forceinline__ void k4_consumer(char* __restrict__ ws, char* smem, unsigned short* __restrict__ out, int mt, int nt, int wave, int lane STAMP_ARG) {
    const int th = wave & 1, kh = wave >> 1;
    const int tok = lane & 15, g = lane >> 4;
    const int boff = 2 * th * 2048 + kh * 1024 + lane * 16;
    const int aoff = 8192 + kh * 5120 + lane * 16;
    f32x4 acc[2][5];
#pragma unroll
    for (int t = 0; t < 2; ++t)
#pragma unroll
        for (int D = 0; D < 5; ++D) acc[t][D] = f32x4{0.f, 0.f, 0.f, 0.f};
    struct Fr { b16x8 a[5], b[2]; };
    auto load = [&](Fr& f, const char* sl) {
#pragma unroll
        for (int t = 0; t < 2; ++t) f.b[t] = *(const b16x8*)(sl + boff + t * 2048);
#pragma unroll
        for (int D = 0; D < 5; ++D) f.a[D] = *(const b16x8*)(sl + aoff + D * 1024);
    };
    auto step = [&](const Fr& c, Fr& n, const char* sl) {
#pragma unroll
        for (int D = 0; D < 5; ++D)
#pragma unroll
            for (int t = 0; t < 2; ++t) {
                acc[t][D] = __builtin_amdgcn_mfma_f32_16x16x32_bf16(c.a[D], c.b[t], acc[t][D], 0, 0, 0);
                const int m = 2 * D + t;
                if (m < 2) n.b[m] = *(const b16x8*)(sl + boff + m * 2048);
                else if (m < 7) n.a[m - 2] = *(const b16x8*)(sl + aoff + (m - 2) * 1024);
                GAP();
            }
    };
    Fr f0, f1;
    asm volatile("s_barrier" ::: "memory");
    asm volatile("s_barrier" ::: "memory");
    STAMP(13);
    load(f0, smem);
    int slot = K4_STAGE;
#pragma unroll 1
    for (int st = 1; st < 27; st += 2) {
        asm volatile("s_waitcnt lgkmcnt(0)\n\ts_barrier" ::: "memory");
        GAP();
        step(f0, f1, smem + slot);
        slot = (slot == (K4_NS - 1) * K4_STAGE) ? 0 : slot + K4_STAGE;
        asm volatile("s_waitcnt lgkmcnt(0)\n\ts_barrier" ::: "memory");
        GAP();
        step(f1, f0, smem + slot);
        slot = (slot == (K4_NS - 1) * K4_STAGE) ? 0 : slot + K4_STAGE;
    }
#pragma unroll
    for (int D = 0; D < 5; ++D)
#pragma unroll
        for (int t = 0; t < 2; ++t) acc[t][D] = __builtin_amdgcn_mfma_f32_16x16x32_bf16(f0.a[D], f0.b[t], acc[t][D], 0, 0, 0);
    asm volatile("" :: "v"(acc[1][4][0])); STAMP(14);
    asm volatile("s_waitcnt lgkmcnt(0)\n\ts_barrier" ::: "memory");
    f32x4* red = (f32x4*)smem;
#pragma unroll
    for (int D = 0; D < 5; ++D) red[((th * 2 + kh) * 5 + D) * 64 + lane] = kh ? acc[0][D] : acc[1][D];
    asm volatile("s_waitcnt lgkmcnt(0)\n\ts_barrier" ::: "memory");
    const int token = 64 * mt + 32 * th + 16 * kh + tok;
    const int q = (lane & 15) >> 2, p = lane & 3;
#pragma unroll
    for (int D = 0; D < 5; ++D) {
        const f32x4 o = red[((th * 2 + (kh ^ 1)) * 5 + D) * 64 + lane];
        const f32x4 m = kh ? acc[1][D] : acc[0][D];
        const s16x4 hr = tr_read(smem + K4_HOFF + ((4 * D + g) * 4 + 2 * th + kh) * 128 + q * 32 + 8 * p);
        const u32x2 hw = bc<u32x2>(hr);
        const float r0 = lo16(hw[0]) + bf16r(m[0] + o[0]), r1 = hi16(hw[0]) + bf16r(m[1] + o[1]);
        const float r2 = lo16(hw[1]) + bf16r(m[2] + o[2]), r3 = hi16(hw[1]) + bf16r(m[3] + o[3]);
        *(u32x2*)(out + (size_t)token * DM + 80 * nt + 16 * D + 4 * g) = u32x2{pk_bf16(r0, r1), pk_bf16(r2, r3)};
    }
    STAMP(15);
}

constexpr int KB_LDS = K4_LDS > K3_LDS ? K4_LDS : K3_LDS;
static_assert(KB_LDS >= K2_LDS && KB_LDS <= 163840, "LDS budget");
constexpr int WARM_CODE = 40960;

template <int NT>
__device__ __forceinline__ void ffn_block(char* __restrict__ ws, char* smem, unsigned short* __restrict__ out, int mt, int sl, int T0,
                                          int mt2, int nt, int wave, int lane, u32 ev STAMP_ARG) {
    if (wave < 4) {
        asm volatile("s_barrier" ::: "memory");
        STAMP(8);
        k3_consumer<NT>(ws, smem, mt, T0, wave, lane STAMP_PASS);
        k4_consumer(ws, smem, out, mt2, nt, wave, lane STAMP_PASS);
    } else {
        const u32 tag = (u32)rfl((int)ev) + TAG_STEP;
        if (wave == 4) {
            const __amdgpu_buffer_rsrc_t srd = make_srd(ws, (u32)WS_END);
            const u32 off = (u32)OFF_F2 + (blockIdx.x & 15) * 1024 + lane * 16;
            for (int spin = 0; spin < (1 << 21); ++spin) {
                const u32x4 f = poll16(srd, off);
                if (__all(f[0] == tag && f[1] == tag && f[2] == tag && f[3] == tag)) break;
                __builtin_amdgcn_s_sleep(POLL_SLEEP);
            }
            if (blockIdx.x == 0 && lane == 0) __hip_atomic_store((gu32*)(ws + OFF_EP), tag, __ATOMIC_RELAXED, __HIP_MEMORY_SCOPE_AGENT);
        }
        asm volatile("s_waitcnt vmcnt(0)\n\ts_barrier" ::: "memory");
        k3_loader<NT>(ws, smem, mt, sl, T0, wave - 4, lane, tag);
        if (wave < 6) k4_loader<3>(ws, smem, mt, sl, mt2, nt, wave - 4, lane, tag);
        else k4_loader<2>(ws, smem, mt, sl, mt2, nt, wave - 4, lane, tag);
    }
}

__global__ __launch_bounds__(512, 2) void s4d_swiglu_block(const u32x4* __restrict__ x, const unsigned short* __restrict__ logA,
                                                           const unsigned short* __restrict__ Aim, const unsigned short* __restrict__ Cre,
                                                           const unsigned short* __restrict__ Cim, const unsigned short* __restrict__ logdt,
                                                           const unsigned short* __restrict__ Dskip, const char* __restrict__ w1,
                                                           const char* __restrict__ w3, const char* __restrict__ w2, char* __restrict__ ws,
                                                           unsigned short* __restrict__ out) {
    extern __shared__ __attribute__((aligned(16))) char smem[];
    const int tid = threadIdx.x, wave = rfl(tid >> 6), lane = tid & 63;
    const int b = blockIdx.x;
    STAMP_DECL; STAMP(0);
    const u32 ev = __hip_atomic_load((const gu32*)(ws + OFF_EP), __ATOMIC_RELAXED, __HIP_MEMORY_SCOPE_AGENT);
    if (b < 160) {
        conv_block(20 * (b & 7) + (b >> 3), (const char*)x, logA, Aim, Cre, Cim, logdt, Dskip, ws, smem, ev STAMP_PASS);
    } else {
        if (b >= 224) {
            unsigned long long pc;
            asm volatile("s_getpc_b64 %0" : "=s"(pc));
            u32 junk = 0;
            if (tid * 128 < WARM_CODE) asm volatile("global_load_dword %0, %1, off" : "+v"(junk) : "v"((const char*)(pc & ~127ull) + tid * 128) : "memory");
            for (int i = 0; i < PREP_DELAY; ++i) __builtin_amdgcn_s_sleep(16);
            asm volatile("s_waitcnt vmcnt(0)" : "+v"(junk) :: "memory");
        } else {
            ln1_block(b - 160, x, ws, ev STAMP_PASS);
        }
        const int task = (b >= 224) ? b - 224 : b - 160 + 32;
        if (task < 54) prep13_block(task, w1, w3, ws, smem);
        else if (task < 81) prep2_block(task - 54, w2, ws, smem);
    }
    STAMP(6);
    asm volatile("s_waitcnt vmcnt(0) lgkmcnt(0)\n\ts_barrier" ::: "memory");
    STAMP(7);
    if (tid >= 256 && tid < 272)
        __hip_atomic_store((gu32*)(ws + OFF_F2) + (tid - 256) * 256 + b, (u32)rfl((int)ev) + TAG_STEP, __ATOMIC_RELAXED, __HIP_MEMORY_SCOPE_AGENT);
    const int xcd = b & 7, jb = b >> 3;
    const int mt = 8 * (xcd & 1) + (jb & 7), sl = 4 * (xcd >> 1) + (jb >> 3);
    const int mt2 = 8 * (xcd >> 1) + (jb & 7), nt = 4 * (xcd & 1) + (jb >> 3);
    if (sl < 12) ffn_block<7>(ws, smem, out, mt, sl, 14 * sl, mt2, nt, wave, lane, ev STAMP_PASS);
    else ffn_block<6>(ws, smem, out, mt, sl, 168 + 12 * (sl - 12), mt2, nt, wave, lane, ev STAMP_PASS);
    STAMP_FLUSH(ws, 0);
}

}

extern "C" void kernel_launch(void* const* d_in, const int* in_sizes, int n_in, void* d_out, int out_size, void* d_ws,
                              size_t ws_size, hipStream_t stream) {
    (void)in_sizes; (void)n_in; (void)out_size; (void)ws_size;
    hipFuncSetAttribute((const void*)s4d_swiglu_block, hipFuncAttributeMaxDynamicSharedMemorySize, KB_LDS);
    s4d_swiglu_block<<<256, 512, KB_LDS, stream>>>((const u32x4*)d_in[0], (const unsigned short*)d_in[1], (const unsigned short*)d_in[2],
                                                   (const unsigned short*)d_in[3], (const unsigned short*)d_in[4],
                                                   (const unsigned short*)d_in[5], (const unsigned short*)d_in[6], (const char*)d_in[8],
                                                   (const char*)d_in[9], (const char*)d_in[10], (char*)d_ws, (unsigned short*)d_out);
}
```
